```python
import torch

def _softmax(x, axis=-1):
    return torch.softmax(x, dim=axis)
def _gelu(x):
    return torch.nn.functional.gelu(x, approximate="tanh")
def _silu(x):
    return torch.nn.functional.silu(x)
def _sigmoid(x):
    return torch.sigmoid(x)
def _layernorm(x, axis=-1, eps=1e-5):
    m = x.mean(dim=axis, keepdim=True)
    v = x.var(dim=axis, keepdim=True, unbiased=False)
    return (x - m) / torch.sqrt(v + eps)
def _scatter_add(tgt, idx, src, axis=0):
    return tgt.clone().index_add_(axis, idx, src)
def _segment_sum(src, seg, n):
    out = torch.zeros((n,) + src.shape[1:], dtype=src.dtype)
    return out.index_add_(0, seg, src)
def _pad(x, pw):
    flat = tuple(v for pair in reversed(pw) for v in pair)
    return torch.nn.functional.pad(x, flat)

kernel_name = 'sequential_preLN__mha_block_cached__geglu_ffn__decode'

def setup_inputs():

    _w = (lambda a: (lambda t: t.to(torch.bfloat16) if t.is_floating_point() else t)(torch.as_tensor(a)))
    def _A_setup():
        B, Sq, D = 2, 1, 448
        H, Sk, Dh = 16, 16384, 256
        return {
            "x": _w((lambda *s: torch.randn(*s, dtype=torch.bfloat16) * 0.05)(B, Sq, D)),
            "k_cache": _w((lambda *s: torch.randn(*s, dtype=torch.bfloat16) * 3.2)(B, H, Sk, Dh)),
            "v_cache": _w((lambda *s: torch.randn(*s, dtype=torch.bfloat16) * 0.4)(B, H, Sk, Dh)),
            "wq": _w((lambda *s: torch.randn(*s, dtype=torch.bfloat16) * 3.2)(D, H * Dh) * (D ** -0.5)),
            "wo": _w((lambda *s: torch.randn(*s, dtype=torch.bfloat16) * 0.4)(H * Dh, D) * ((H * Dh) ** -0.5)),
        }
    _A_in = _A_setup()

    def _B_setup():
        B, S, D, F = 2, 1, 448, 1792
        return {
            "x": _w((lambda *s: torch.randn(*s, dtype=torch.bfloat16) * 0.05)(B, S, D)),
            "w_gate": _w((lambda *s: torch.randn(*s, dtype=torch.bfloat16) * 0.05)(D, F)),
            "w_up": _w((lambda *s: torch.randn(*s, dtype=torch.bfloat16) * 0.05)(D, F)),
            "w_down": _w((lambda *s: torch.randn(*s, dtype=torch.bfloat16) * 0.05)(F, D)),
        }
    _B_in = _B_setup()

    return {
        "A_x": _A_in["x"],
        "A_k_cache": _A_in["k_cache"],
        "A_v_cache": _A_in["v_cache"],
        "A_wq": _A_in["wq"],
        "A_wo": _A_in["wo"],
        "B_x": _B_in["x"],
        "B_w_gate": _B_in["w_gate"],
        "B_w_up": _B_in["w_up"],
        "B_w_down": _B_in["w_down"],
    }

def reference(A_x, A_k_cache, A_v_cache, A_wq, A_wo, B_x, B_w_gate, B_w_up, B_w_down):
    def _A_ref(x, k_cache, v_cache, wq, wo):
        B, Sq, D = x.shape
        H, Dh = 16, 256
        q = (lambda x, *d: x.permute(*d))((x @ wq).reshape(B, Sq, H, Dh), 0, 2, 1, 3)
        scores = torch.einsum("bhqd,bhkd->bhqk", q, k_cache) * (Dh ** -0.5)
        p = _softmax(scores, axis=-1)
        o = torch.einsum("bhqk,bhkd->bhqd", p, v_cache)
        return (lambda x, *d: x.permute(*d))(o, 0, 2, 1, 3).reshape(B, Sq, H * Dh) @ wo

    def _B_ref(x, w_gate, w_up, w_down):
        return (_gelu(x @ w_gate) * (x @ w_up)) @ w_down

    x = A_x
    h = x + _A_ref(_layernorm(x, axis=-1), A_k_cache, A_v_cache, A_wq, A_wo)
    h2 = h + _B_ref(_layernorm(h, axis=-1), B_w_gate, B_w_up, B_w_down)
    return h2
```

```cpp
#include <hip/hip_runtime.h>
#include <hip/hip_bf16.h>
#include <stdint.h>

#define MIMIC 1

typedef unsigned short bf16_t;
typedef unsigned u32x4 __attribute__((ext_vector_type(4)));
typedef float f32x4 __attribute__((ext_vector_type(4)));
typedef __bf16 bf16x2 __attribute__((ext_vector_type(2)));

constexpr int B = 2, D = 448, H = 16, SK = 16384, DH = 256, HD = H * DH, FF = 1792;
constexpr int NSPLIT = 8, NWAVE = 8, NTHR = NWAVE * 64;
constexpr int KEYS_PER_WG = SK / NSPLIT;

__device__ __forceinline__ float bf2f(unsigned short v) { return __builtin_bit_cast(float, (unsigned)v << 16); }
__device__ __forceinline__ float bflo(unsigned v) { return __builtin_bit_cast(float, v << 16); }
__device__ __forceinline__ float bfhi(unsigned v) { return __builtin_bit_cast(float, v & 0xffff0000u); }
__device__ __forceinline__ unsigned short f2bf(float f) {
  __bf16 b = (__bf16)f;
  return __builtin_bit_cast(unsigned short, b);
}
__device__ __forceinline__ float rbf(float f) {
  return bf2f(f2bf(f));
}
__device__ __forceinline__ float dot2(unsigned k, unsigned q, float acc) {
  return __builtin_amdgcn_fdot2_f32_bf16(__builtin_bit_cast(bf16x2, k), __builtin_bit_cast(bf16x2, q), acc, false);
}
template <int CTRL> __device__ __forceinline__ float dpp(float x) {
  return __builtin_bit_cast(float, __builtin_amdgcn_mov_dpp(__builtin_bit_cast(int, x), CTRL, 0xf, 0xf, true));
}
constexpr int XOR1 = 0xB1, XOR2 = 0x4E, HMIR = 0x141, MIR = 0x140;
__device__ __forceinline__ float row16_sum(float x) {
  x += dpp<XOR1>(x); x += dpp<XOR2>(x); x += dpp<HMIR>(x); x += dpp<MIR>(x);
  return x;
}
__device__ __forceinline__ float wave_sum(float x) {
  for (int m = 32; m >= 1; m >>= 1) x += __shfl_xor(x, m);
  return x;
}

__device__ __forceinline__ void ln_wave(const float* in, float* out, int lane) {
  float v[7]; float s = 0.f;
#pragma unroll
  for (int i = 0; i < 7; ++i) { v[i] = in[lane + 64 * i]; s += v[i]; }
  s = wave_sum(s);
  const float mean = s * (1.f / D);
  float q = 0.f;
#pragma unroll
  for (int i = 0; i < 7; ++i) { const float d = v[i] - mean; q += d * d; }
  q = wave_sum(q);
  const float var = q * (1.f / D);
  const float mb = rbf(mean), vb = rbf(var);
  const float sd = rbf(sqrtf(rbf(vb + 1e-5f)));
#pragma unroll
  for (int i = 0; i < 7; ++i) out[lane + 64 * i] = rbf(rbf(v[i] - mb) / sd);
}

__global__ void __launch_bounds__(256) k_qproj(const bf16_t* __restrict__ x, const bf16_t* __restrict__ wq, bf16_t* __restrict__ qout) {
  __shared__ float xs[2][D];
  __shared__ float red[32][2][64];
  const int tid = threadIdx.x, lane = tid & 63, wave = tid >> 6;
  for (int i = tid; i < 2 * D; i += 256) (&xs[0][0])[i] = bf2f(x[i]);
  __syncthreads();
  if (wave < 2) ln_wave(xs[wave], xs[wave], lane);
  __syncthreads();
  const int cg = tid & 7, ks = tid >> 3;
  const int c0 = blockIdx.x * 64 + cg * 8;
  float a0[8] = {0}, a1[8] = {0};
#pragma unroll
  for (int r = 0; r < 14; ++r) {
    const int k = ks * 14 + r;
    const u32x4 w = *(const u32x4*)(wq + (size_t)k * HD + c0);
    const float x0 = xs[0][k], x1 = xs[1][k];
#pragma unroll
    for (int j = 0; j < 4; ++j) {
      a0[2 * j] = fmaf(x0, bflo(w[j]), a0[2 * j]); a0[2 * j + 1] = fmaf(x0, bfhi(w[j]), a0[2 * j + 1]);
      a1[2 * j] = fmaf(x1, bflo(w[j]), a1[2 * j]); a1[2 * j + 1] = fmaf(x1, bfhi(w[j]), a1[2 * j + 1]);
    }
  }
#pragma unroll
  for (int j = 0; j < 8; ++j) { red[ks][0][cg * 8 + j] = a0[j]; red[ks][1][cg * 8 + j] = a1[j]; }
  __syncthreads();
  if (tid < 128) {
    const int b = tid >> 6, c = tid & 63;
    float s = 0.f;
    for (int k = 0; k < 32; ++k) s += red[k][b][c];
    qout[b * HD + blockIdx.x * 64 + c] = f2bf(s);
  }
}

constexpr int PREC = 260;
__global__ void __launch_bounds__(NTHR, 2) k_attn(const bf16_t* __restrict__ kc, const bf16_t* __restrict__ vc,
                                                  const bf16_t* __restrict__ q, float* __restrict__ part) {
  __shared__ __attribute__((aligned(16))) float mrg[NWAVE * 4][PREC];
  const int tid = threadIdx.x, lane = tid & 63, wave = __builtin_amdgcn_readfirstlane(tid >> 6);
  const int r = lane >> 4, c = lane & 15;
  const int bh = blockIdx.x / NSPLIT, sp = blockIdx.x % NSPLIT;
  const int b = bh / H, h = bh % H;
  const u32x4 q0 = *(const u32x4*)(q + b * HD + h * DH + 8 * c);
  const u32x4 q1 = *(const u32x4*)(q + b * HD + h * DH + 128 + 8 * c);
  const size_t base = ((size_t)bh * SK + (size_t)sp * KEYS_PER_WG + wave * 8 + r) * DH + 8 * c;
  const bf16_t* kp = kc + base;
  const bf16_t* vp = vc + base;
  constexpr int NT = KEYS_PER_WG / (8 * NWAVE);
  constexpr int TSTR = 8 * NWAVE * DH;
  constexpr int DEPTH = 4;
  u32x4 kb[DEPTH][4], vb[DEPTH][4];
#define LD(p) __builtin_nontemporal_load((const u32x4*)(p))
#define ISSUE(s, t) do { const bf16_t* k_ = kp + (size_t)(t) * TSTR; const bf16_t* v_ = vp + (size_t)(t) * TSTR; \
    kb[s][0] = LD(k_); kb[s][1] = LD(k_ + 128); kb[s][2] = LD(k_ + 4 * DH); kb[s][3] = LD(k_ + 4 * DH + 128); \
    vb[s][0] = LD(v_); vb[s][1] = LD(v_ + 128); vb[s][2] = LD(v_ + 4 * DH); vb[s][3] = LD(v_ + 4 * DH + 128); } while (0)
  float m = -INFINITY, l = 0.f, acc[16];
#pragma unroll
  for (int i = 0; i < 16; ++i) acc[i] = 0.f;
  constexpr float C2 = 0.0625f * 1.4426950408889634f;
#pragma unroll
  for (int s = 0; s < DEPTH; ++s) ISSUE(s, s);
  __builtin_amdgcn_sched_barrier(0);
  for (int t0 = 0; t0 < NT; t0 += DEPTH) {
#pragma unroll
    for (int s = 0; s < DEPTH; ++s) {
      float s0 = 0.f, s1 = 0.f;
#pragma unroll
      for (int j = 0; j < 4; ++j) { s0 = dot2(kb[s][0][j], q0[j], s0); s1 = dot2(kb[s][2][j], q0[j], s1); }
#pragma unroll
      for (int j = 0; j < 4; ++j) { s0 = dot2(kb[s][1][j], q1[j], s0); s1 = dot2(kb[s][3][j], q1[j], s1); }
      s0 = row16_sum(s0); s1 = row16_sum(s1);
      s0 = rbf(s0) * C2; s1 = rbf(s1) * C2;
      const float mn = fmaxf(m, fmaxf(s0, s1));
      const float al = __builtin_amdgcn_exp2f(m - mn);
      const float p0 = __builtin_amdgcn_exp2f(s0 - mn), p1 = __builtin_amdgcn_exp2f(s1 - mn);
      l = fmaf(l, al, p0 + p1); m = mn;
#pragma unroll
      for (int j = 0; j < 4; ++j) {
        acc[2 * j] = fmaf(p1, bflo(vb[s][2][j]), fmaf(p0, bflo(vb[s][0][j]), acc[2 * j] * al));
        acc[2 * j + 1] = fmaf(p1, bfhi(vb[s][2][j]), fmaf(p0, bfhi(vb[s][0][j]), acc[2 * j + 1] * al));
        acc[8 + 2 * j] = fmaf(p1, bflo(vb[s][3][j]), fmaf(p0, bflo(vb[s][1][j]), acc[8 + 2 * j] * al));
        acc[8 + 2 * j + 1] = fmaf(p1, bfhi(vb[s][3][j]), fmaf(p0, bfhi(vb[s][1][j]), acc[8 + 2 * j + 1] * al));
      }
      __builtin_amdgcn_sched_barrier(0);
      if (t0 + DEPTH + s < NT) ISSUE(s, t0 + DEPTH + s);
      __builtin_amdgcn_sched_barrier(0);
    }
  }
  float* rec = mrg[wave * 4 + r];
  *(f32x4*)(rec + 8 * c) = f32x4{acc[0], acc[1], acc[2], acc[3]};
  *(f32x4*)(rec + 8 * c + 4) = f32x4{acc[4], acc[5], acc[6], acc[7]};
  *(f32x4*)(rec + 128 + 8 * c) = f32x4{acc[8], acc[9], acc[10], acc[11]};
  *(f32x4*)(rec + 128 + 8 * c + 4) = f32x4{acc[12], acc[13], acc[14], acc[15]};
  if (c == 0) { rec[256] = m; rec[257] = l; }
  __syncthreads();
  if (tid < 256) {
    float M = -INFINITY;
    for (int i = 0; i < NWAVE * 4; ++i) M = fmaxf(M, mrg[i][256]);
    float L = 0.f, o = 0.f;
    for (int i = 0; i < NWAVE * 4; ++i) {
      const float w = __builtin_amdgcn_exp2f(mrg[i][256] - M);
      L = fmaf(w, mrg[i][257], L); o = fmaf(w, mrg[i][tid], o);
    }
    float* pr = part + (size_t)blockIdx.x * PREC;
    pr[tid] = o;
    if (tid == 0) { pr[256] = M; pr[257] = L; }
  }
}

__global__ void __launch_bounds__(256) k_wo(const float* __restrict__ part, const bf16_t* __restrict__ wo, float* __restrict__ apart) {
  __shared__ float os[2][64];
  __shared__ float red[4][2][D];
  const int tid = threadIdx.x, j = blockIdx.x;
  const int h = j / 4, d0 = (j % 4) * 64;
  if (tid < 128) {
    const int b = tid >> 6, d = d0 + (tid & 63);
    const float* p = part + (size_t)((b * H + h) * NSPLIT) * PREC;
    float M = -INFINITY;
    for (int s = 0; s < NSPLIT; ++s) M = fmaxf(M, p[s * PREC + 256]);
    float L = 0.f, o = 0.f;
    for (int s = 0; s < NSPLIT; ++s) {
      const float w = __builtin_amdgcn_exp2f(p[s * PREC + 256] - M);
      L = fmaf(w, p[s * PREC + 257], L); o = fmaf(w, p[s * PREC + d], o);
    }
    os[b][tid & 63] = rbf(o / L);
  }
  __syncthreads();
  if (tid < 224) {
    const int cg = tid % 56, ks = tid / 56;
    float a0[8] = {0}, a1[8] = {0};
#pragma unroll
    for (int r = 0; r < 16; ++r) {
      const int k = ks * 16 + r;
      const u32x4 w = *(const u32x4*)(wo + (size_t)(j * 64 + k) * D + cg * 8);
      const float x0 = os[0][k], x1 = os[1][k];
#pragma unroll
      for (int i = 0; i < 4; ++i) {
        a0[2 * i] = fmaf(x0, bflo(w[i]), a0[2 * i]); a0[2 * i + 1] = fmaf(x0, bfhi(w[i]), a0[2 * i + 1]);
        a1[2 * i] = fmaf(x1, bflo(w[i]), a1[2 * i]); a1[2 * i + 1] = fmaf(x1, bfhi(w[i]), a1[2 * i + 1]);
      }
    }
#pragma unroll
    for (int i = 0; i < 8; ++i) { red[ks][0][cg * 8 + i] = a0[i]; red[ks][1][cg * 8 + i] = a1[i]; }
  }
  __syncthreads();
  for (int i = tid; i < 2 * D; i += 256) {
    const int b = i / D, c = i % D;
    apart[(size_t)j * 2 * D + i] = red[0][b][c] + red[1][b][c] + red[2][b][c] + red[3][b][c];
  }
}

__device__ __forceinline__ float gelu_tanh(float x) {
  const float u = 0.7978845608028654f * (x + 0.044715f * x * x * x);
  return 0.5f * x * (1.f + tanhf(u));
}

__global__ void __launch_bounds__(256) k_gateup(const bf16_t* __restrict__ x, const float* __restrict__ apart,
                                                const bf16_t* __restrict__ wg, const bf16_t* __restrict__ wu,
                                                float* __restrict__ hbuf, float* __restrict__ gbuf) {
  __shared__ float hs[2][D];
  __shared__ float red[32][2][2][32];
  const int tid = threadIdx.x, lane = tid & 63, wave = tid >> 6;
  for (int i = tid; i < 2 * D; i += 256) {
    float s = 0.f;
    for (int j = 0; j < 64; ++j) s += apart[(size_t)j * 2 * D + i];
    const float hv = rbf(bf2f(x[i]) + rbf(s));
    (&hs[0][0])[i] = hv;
    if (blockIdx.x == 0) hbuf[i] = hv;
  }
  __syncthreads();
  if (wave < 2) ln_wave(hs[wave], hs[wave], lane);
  __syncthreads();
  const int mat = tid & 1, cg = (tid >> 1) & 3, ks = tid >> 3;
  const bf16_t* w_ = mat ? wu : wg;
  const int c0 = blockIdx.x * 32 + cg * 8;
  float a0[8] = {0}, a1[8] = {0};
#pragma unroll
  for (int r = 0; r < 14; ++r) {
    const int k = ks * 14 + r;
    const u32x4 w = *(const u32x4*)(w_ + (size_t)k * FF + c0);
    const float x0 = hs[0][k], x1 = hs[1][k];
#pragma unroll
    for (int j = 0; j < 4; ++j) {
      a0[2 * j] = fmaf(x0, bflo(w[j]), a0[2 * j]); a0[2 * j + 1] = fmaf(x0, bfhi(w[j]), a0[2 * j + 1]);
      a1[2 * j] = fmaf(x1, bflo(w[j]), a1[2 * j]); a1[2 * j + 1] = fmaf(x1, bfhi(w[j]), a1[2 * j + 1]);
    }
  }
#pragma unroll
  for (int j = 0; j < 8; ++j) { red[ks][mat][0][cg * 8 + j] = a0[j]; red[ks][mat][1][cg * 8 + j] = a1[j]; }
  __syncthreads();
  if (tid < 64) {
    const int b = tid >> 5, c = tid & 31;
    float sg = 0.f, su = 0.f;
    for (int k = 0; k < 32; ++k) { sg += red[k][0][b][c]; su += red[k][1][b][c]; }
    gbuf[b * FF + blockIdx.x * 32 + c] = rbf(rbf(gelu_tanh(rbf(sg))) * rbf(su));
  }
}

__global__ void __launch_bounds__(256) k_down(const float* __restrict__ hbuf, const float* __restrict__ gbuf,
                                              const bf16_t* __restrict__ wd, bf16_t* __restrict__ out) {
  __shared__ float red[4][16];
  const int tid = threadIdx.x, lane = tid & 63, wave = tid >> 6;
  const int c0 = blockIdx.x * 8;
  float a[16];
#pragma unroll
  for (int i = 0; i < 16; ++i) a[i] = 0.f;
#pragma unroll
  for (int r = 0; r < 7; ++r) {
    const int k = tid + 256 * r;
    const u32x4 w = *(const u32x4*)(wd + (size_t)k * D + c0);
    const float x0 = gbuf[k], x1 = gbuf[FF + k];
#pragma unroll
    for (int j = 0; j < 4; ++j) {
      a[2 * j] = fmaf(x0, bflo(w[j]), a[2 * j]); a[2 * j + 1] = fmaf(x0, bfhi(w[j]), a[2 * j + 1]);
      a[8 + 2 * j] = fmaf(x1, bflo(w[j]), a[8 + 2 * j]); a[8 + 2 * j + 1] = fmaf(x1, bfhi(w[j]), a[8 + 2 * j + 1]);
    }
  }
#pragma unroll
  for (int i = 0; i < 16; ++i) { a[i] = wave_sum(a[i]); }
  if (lane == 0) {
#pragma unroll
    for (int i = 0; i < 16; ++i) red[wave][i] = a[i];
  }
  __syncthreads();
  if (tid < 16) {
    const int b = tid >> 3, c = c0 + (tid & 7);
    const float s = red[0][tid] + red[1][tid] + red[2][tid] + red[3][tid];
    out[b * D + c] = f2bf(hbuf[b * D + c] + rbf(s));
  }
}

extern "C" void kernel_launch(void* const* d_in, const int* in_sizes, int n_in, void* d_out, int out_size, void* d_ws,
                              size_t ws_size, hipStream_t stream) {
  const bf16_t* x = (const bf16_t*)d_in[0];
  const bf16_t* kc = (const bf16_t*)d_in[1];
  const bf16_t* vc = (const bf16_t*)d_in[2];
  const bf16_t* wq = (const bf16_t*)d_in[3];
  const bf16_t* wo = (const bf16_t*)d_in[4];
  const bf16_t* wg = (const bf16_t*)d_in[6];
  const bf16_t* wu = (const bf16_t*)d_in[7];
  const bf16_t* wd = (const bf16_t*)d_in[8];
  char* ws = (char*)d_ws;
  bf16_t* q = (bf16_t*)ws;
  float* part = (float*)(ws + 65536);
  float* apart = (float*)(ws + 65536 + 524288);
  float* hbuf = (float*)(ws + 1048576);
  float* gbuf = (float*)(ws + 1048576 + 65536);
  k_qproj<<<64, 256, 0, stream>>>(x, wq, q);
  k_attn<<<B * H * NSPLIT, NTHR, 0, stream>>>(kc, vc, q, part);
  k_wo<<<64, 256, 0, stream>>>(part, wo, apart);
  k_gateup<<<56, 256, 0, stream>>>(x, apart, wg, wu, hbuf, gbuf);
  k_down<<<56, 256, 0, stream>>>(hbuf, gbuf, wd, (bf16_t*)d_out);
}
```

```cpp
#include <hip/hip_runtime.h>
#include <hip/hip_bf16.h>
#include <stdint.h>

#define MIMIC 1

typedef unsigned short bf16_t;
typedef unsigned u32x4 __attribute__((ext_vector_type(4)));
typedef float f32x4 __attribute__((ext_vector_type(4)));
typedef __bf16 bf16x2 __attribute__((ext_vector_type(2)));

constexpr int B = 2, D = 448, H = 16, SK = 16384, DH = 256, HD = H * DH, FF = 1792;
constexpr int NSPLIT = 8, NWAVE = 8, NTHR = NWAVE * 64;
constexpr int KEYS_PER_WG = SK / NSPLIT;

__device__ __forceinline__ float bf2f(unsigned short v) { return __builtin_bit_cast(float, (unsigned)v << 16); }
__device__ __forceinline__ float bflo(unsigned v) { return __builtin_bit_cast(float, v << 16); }
__device__ __forceinline__ float bfhi(unsigned v) { return __builtin_bit_cast(float, v & 0xffff0000u); }
__device__ __forceinline__ unsigned short f2bf(float f) {
  __bf16 b = (__bf16)f;
  return __builtin_bit_cast(unsigned short, b);
}
__device__ __forceinline__ float rbf(float f) {
  return bf2f(f2bf(f));
}
__device__ __forceinline__ float dot2(unsigned k, unsigned q, float acc) {
  return __builtin_amdgcn_fdot2_f32_bf16(__builtin_bit_cast(bf16x2, k), __builtin_bit_cast(bf16x2, q), acc, false);
}
template <int CTRL> __device__ __forceinline__ float dpp(float x) {
  return __builtin_bit_cast(float, __builtin_amdgcn_mov_dpp(__builtin_bit_cast(int, x), CTRL, 0xf, 0xf, true));
}
constexpr int XOR1 = 0xB1, XOR2 = 0x4E, HMIR = 0x141, MIR = 0x140;
__device__ __forceinline__ float row16_sum(float x) {
  x += dpp<XOR1>(x); x += dpp<XOR2>(x); x += dpp<HMIR>(x); x += dpp<MIR>(x);
  return x;
}
__device__ __forceinline__ float wave_sum(float x) {
  for (int m = 32; m >= 1; m >>= 1) x += __shfl_xor(x, m);
  return x;
}

__device__ __forceinline__ void ln_wave(const float* in, float* out, int lane) {
  float v[7]; float s = 0.f;
#pragma unroll
  for (int i = 0; i < 7; ++i) { v[i] = in[lane + 64 * i]; s += v[i]; }
  s = wave_sum(s);
  const float mean = s * (1.f / D);
  float q = 0.f;
#pragma unroll
  for (int i = 0; i < 7; ++i) { const float d = v[i] - mean; q += d * d; }
  q = wave_sum(q);
  const float var = q * (1.f / D);
  const float mb = rbf(mean), vb = rbf(var);
  const float sd = rbf(sqrtf(rbf(vb + 1e-5f)));
#pragma unroll
  for (int i = 0; i < 7; ++i) out[lane + 64 * i] = rbf(rbf(v[i] - mb) / sd);
}

__global__ void __launch_bounds__(256) k_qproj(const bf16_t* __restrict__ x, const bf16_t* __restrict__ wq, bf16_t* __restrict__ qout) {
  __shared__ float xs[2][D];
  __shared__ float red[32][2][64];
  const int tid = threadIdx.x, lane = tid & 63, wave = tid >> 6;
  for (int i = tid; i < 2 * D; i += 256) (&xs[0][0])[i] = bf2f(x[i]);
  __syncthreads();
  if (wave < 2) ln_wave(xs[wave], xs[wave], lane);
  __syncthreads();
  const int cg = tid & 7, ks = tid >> 3;
  const int c0 = blockIdx.x * 64 + cg * 8;
  float a0[8] = {0}, a1[8] = {0};
#pragma unroll
  for (int r = 0; r < 14; ++r) {
    const int k = ks * 14 + r;
    const u32x4 w = *(const u32x4*)(wq + (size_t)k * HD + c0);
    const float x0 = xs[0][k], x1 = xs[1][k];
#pragma unroll
    for (int j = 0; j < 4; ++j) {
      a0[2 * j] = fmaf(x0, bflo(w[j]), a0[2 * j]); a0[2 * j + 1] = fmaf(x0, bfhi(w[j]), a0[2 * j + 1]);
      a1[2 * j] = fmaf(x1, bflo(w[j]), a1[2 * j]); a1[2 * j + 1] = fmaf(x1, bfhi(w[j]), a1[2 * j + 1]);
    }
  }
#pragma unroll
  for (int j = 0; j < 8; ++j) { red[ks][0][cg * 8 + j] = a0[j]; red[ks][1][cg * 8 + j] = a1[j]; }
  __syncthreads();
  if (tid < 128) {
    const int b = tid >> 6, c = tid & 63;
    float s = 0.f;
    for (int k = 0; k < 32; ++k) s += red[k][b][c];
    qout[b * HD + blockIdx.x * 64 + c] = f2bf(s);
  }
}

constexpr int PREC = 260;
__global__ void __launch_bounds__(NTHR, 2) k_attn(const bf16_t* __restrict__ kc, const bf16_t* __restrict__ vc,
                                                  const bf16_t* __restrict__ q, float* __restrict__ part) {
  __shared__ __attribute__((aligned(16))) float mrg[NWAVE * 4][PREC];
  const int tid = threadIdx.x, lane = tid & 63, wave = __builtin_amdgcn_readfirstlane(tid >> 6);
  const int r = lane >> 4, c = lane & 15;
  const int bh = blockIdx.x / NSPLIT, sp = blockIdx.x % NSPLIT;
  const int b = bh / H, h = bh % H;
  const u32x4 q0 = *(const u32x4*)(q + b * HD + h * DH + 8 * c);
  const u32x4 q1 = *(const u32x4*)(q + b * HD + h * DH + 128 + 8 * c);
  const size_t base = ((size_t)bh * SK + (size_t)sp * KEYS_PER_WG + wave * 8) * DH * 2;
  const char* kp = (const char*)kc + base;
  const char* vp = (const char*)vc + base;
  const unsigned loff = r * 512 + c * 16;
  constexpr int NT = KEYS_PER_WG / (8 * NWAVE);
  constexpr int TSTR = 8 * NWAVE * DH * 2;
  constexpr int DEPTH = 4;
  u32x4 kb[DEPTH][4], vb[DEPTH][4];
#define LD(d, sb, OFF) asm volatile("global_load_dwordx4 %0, %1, %2 offset:" #OFF " nt" : "=v"(d) : "v"(loff), "s"(sb))
#define ISSUE(s) do { LD(kb[s][0], kp, 0); LD(kb[s][1], kp, 256); LD(kb[s][2], kp, 2048); LD(kb[s][3], kp, 2304); \
    LD(vb[s][0], vp, 0); LD(vb[s][1], vp, 256); LD(vb[s][2], vp, 2048); LD(vb[s][3], vp, 2304); kp += TSTR; vp += TSTR; } while (0)
#define WAITK(s, N) asm volatile("s_waitcnt vmcnt(%4)" : "+v"(kb[s][0]), "+v"(kb[s][1]), "+v"(kb[s][2]), "+v"(kb[s][3]) : "i"(N))
#define WAITV(s, N) asm volatile("s_waitcnt vmcnt(%4)" : "+v"(vb[s][0]), "+v"(vb[s][1]), "+v"(vb[s][2]), "+v"(vb[s][3]) : "i"(N))
  float m = -INFINITY, l = 0.f, acc[16];
#pragma unroll
  for (int i = 0; i < 16; ++i) acc[i] = 0.f;
  constexpr float C2 = 0.0625f * 1.4426950408889634f;
  u32x4 q0v = q0, q1v = q1;
  asm volatile("s_waitcnt vmcnt(0)" : "+v"(q0v), "+v"(q1v));
#pragma unroll
  for (int s = 0; s < DEPTH; ++s) ISSUE(s);
#define COMPUTE(s, NREM) do { \
      WAITK(s, (NREM) + 4); \
      float s0 = 0.f, s1 = 0.f; \
      _Pragma("unroll") for (int j = 0; j < 4; ++j) { s0 = dot2(kb[s][0][j], q0v[j], s0); s1 = dot2(kb[s][2][j], q0v[j], s1); } \
      _Pragma("unroll") for (int j = 0; j < 4; ++j) { s0 = dot2(kb[s][1][j], q1v[j], s0); s1 = dot2(kb[s][3][j], q1v[j], s1); } \
      s0 = row16_sum(s0); s1 = row16_sum(s1); \
      s0 = rbf(s0) * C2; s1 = rbf(s1) * C2; \
      const float mn = fmaxf(m, fmaxf(s0, s1)); \
      const float al = __builtin_amdgcn_exp2f(m - mn); \
      const float p0 = __builtin_amdgcn_exp2f(s0 - mn), p1 = __builtin_amdgcn_exp2f(s1 - mn); \
      l = fmaf(l, al, p0 + p1); m = mn; \
      WAITV(s, (NREM)); \
      _Pragma("unroll") for (int j = 0; j < 4; ++j) { \
        acc[2 * j] = fmaf(p1, bflo(vb[s][2][j]), fmaf(p0, bflo(vb[s][0][j]), acc[2 * j] * al)); \
        acc[2 * j + 1] = fmaf(p1, bfhi(vb[s][2][j]), fmaf(p0, bfhi(vb[s][0][j]), acc[2 * j + 1] * al)); \
        acc[8 + 2 * j] = fmaf(p1, bflo(vb[s][3][j]), fmaf(p0, bflo(vb[s][1][j]), acc[8 + 2 * j] * al)); \
        acc[8 + 2 * j + 1] = fmaf(p1, bfhi(vb[s][3][j]), fmaf(p0, bfhi(vb[s][1][j]), acc[8 + 2 * j + 1] * al)); \
      } \
      _Pragma("unroll") for (int j = 0; j < 16; ++j) asm volatile("" : "+v"(acc[j])); \
    } while (0)
  for (int t0 = 0; t0 < NT - DEPTH; t0 += DEPTH) {
#pragma unroll
    for (int s = 0; s < DEPTH; ++s) {
      COMPUTE(s, (DEPTH - 1) * 8);
      ISSUE(s);
    }
  }
  COMPUTE(0, 24); COMPUTE(1, 16); COMPUTE(2, 8); COMPUTE(3, 0);
  float* rec = mrg[wave * 4 + r];
  *(f32x4*)(rec + 8 * c) = f32x4{acc[0], acc[1], acc[2], acc[3]};
  *(f32x4*)(rec + 8 * c + 4) = f32x4{acc[4], acc[5], acc[6], acc[7]};
  *(f32x4*)(rec + 128 + 8 * c) = f32x4{acc[8], acc[9], acc[10], acc[11]};
  *(f32x4*)(rec + 128 + 8 * c + 4) = f32x4{acc[12], acc[13], acc[14], acc[15]};
  if (c == 0) { rec[256] = m; rec[257] = l; }
  __syncthreads();
  if (tid < 256) {
    float M = -INFINITY;
    for (int i = 0; i < NWAVE * 4; ++i) M = fmaxf(M, mrg[i][256]);
    float L = 0.f, o = 0.f;
    for (int i = 0; i < NWAVE * 4; ++i) {
      const float w = __builtin_amdgcn_exp2f(mrg[i][256] - M);
      L = fmaf(w, mrg[i][257], L); o = fmaf(w, mrg[i][tid], o);
    }
    float* pr = part + (size_t)blockIdx.x * PREC;
    pr[tid] = o;
    if (tid == 0) { pr[256] = M; pr[257] = L; }
  }
}

__global__ void __launch_bounds__(256) k_wo(const float* __restrict__ part, const bf16_t* __restrict__ wo, float* __restrict__ apart) {
  __shared__ float os[2][64];
  __shared__ float red[4][2][D];
  const int tid = threadIdx.x, j = blockIdx.x;
  const int h = j / 4, d0 = (j % 4) * 64;
  if (tid < 128) {
    const int b = tid >> 6, d = d0 + (tid & 63);
    const float* p = part + (size_t)((b * H + h) * NSPLIT) * PREC;
    float M = -INFINITY;
    for (int s = 0; s < NSPLIT; ++s) M = fmaxf(M, p[s * PREC + 256]);
    float L = 0.f, o = 0.f;
    for (int s = 0; s < NSPLIT; ++s) {
      const float w = __builtin_amdgcn_exp2f(p[s * PREC + 256] - M);
      L = fmaf(w, p[s * PREC + 257], L); o = fmaf(w, p[s * PREC + d], o);
    }
    os[b][tid & 63] = rbf(o / L);
  }
  __syncthreads();
  if (tid < 224) {
    const int cg = tid % 56, ks = tid / 56;
    float a0[8] = {0}, a1[8] = {0};
#pragma unroll
    for (int r = 0; r < 16; ++r) {
      const int k = ks * 16 + r;
      const u32x4 w = *(const u32x4*)(wo + (size_t)(j * 64 + k) * D + cg * 8);
      const float x0 = os[0][k], x1 = os[1][k];
#pragma unroll
      for (int i = 0; i < 4; ++i) {
        a0[2 * i] = fmaf(x0, bflo(w[i]), a0[2 * i]); a0[2 * i + 1] = fmaf(x0, bfhi(w[i]), a0[2 * i + 1]);
        a1[2 * i] = fmaf(x1, bflo(w[i]), a1[2 * i]); a1[2 * i + 1] = fmaf(x1, bfhi(w[i]), a1[2 * i + 1]);
      }
    }
#pragma unroll
    for (int i = 0; i < 8; ++i) { red[ks][0][cg * 8 + i] = a0[i]; red[ks][1][cg * 8 + i] = a1[i]; }
  }
  __syncthreads();
  for (int i = tid; i < 2 * D; i += 256) {
    const int b = i / D, c = i % D;
    apart[(size_t)j * 2 * D + i] = red[0][b][c] + red[1][b][c] + red[2][b][c] + red[3][b][c];
  }
}

__device__ __forceinline__ float gelu_tanh(float x) {
  const float u = 0.7978845608028654f * (x + 0.044715f * x * x * x);
  return 0.5f * x * (1.f + tanhf(u));
}

__global__ void __launch_bounds__(256) k_gateup(const bf16_t* __restrict__ x, const float* __restrict__ apart,
                                                const bf16_t* __restrict__ wg, const bf16_t* __restrict__ wu,
                                                float* __restrict__ hbuf, float* __restrict__ gbuf) {
  __shared__ float hs[2][D];
  __shared__ float red[32][2][2][32];
  const int tid = threadIdx.x, lane = tid & 63, wave = tid >> 6;
  for (int i = tid; i < 2 * D; i += 256) {
    float s = 0.f;
    for (int j = 0; j < 64; ++j) s += apart[(size_t)j * 2 * D + i];
    const float hv = rbf(bf2f(x[i]) + rbf(s));
    (&hs[0][0])[i] = hv;
    if (blockIdx.x == 0) hbuf[i] = hv;
  }
  __syncthreads();
  if (wave < 2) ln_wave(hs[wave], hs[wave], lane);
  __syncthreads();
  const int mat = tid & 1, cg = (tid >> 1) & 3, ks = tid >> 3;
  const bf16_t* w_ = mat ? wu : wg;
  const int c0 = blockIdx.x * 32 + cg * 8;
  float a0[8] = {0}, a1[8] = {0};
#pragma unroll
  for (int r = 0; r < 14; ++r) {
    const int k = ks * 14 + r;
    const u32x4 w = *(const u32x4*)(w_ + (size_t)k * FF + c0);
    const float x0 = hs[0][k], x1 = hs[1][k];
#pragma unroll
    for (int j = 0; j < 4; ++j) {
      a0[2 * j] = fmaf(x0, bflo(w[j]), a0[2 * j]); a0[2 * j + 1] = fmaf(x0, bfhi(w[j]), a0[2 * j + 1]);
      a1[2 * j] = fmaf(x1, bflo(w[j]), a1[2 * j]); a1[2 * j + 1] = fmaf(x1, bfhi(w[j]), a1[2 * j + 1]);
    }
  }
#pragma unroll
  for (int j = 0; j < 8; ++j) { red[ks][mat][0][cg * 8 + j] = a0[j]; red[ks][mat][1][cg * 8 + j] = a1[j]; }
  __syncthreads();
  if (tid < 64) {
    const int b = tid >> 5, c = tid & 31;
    float sg = 0.f, su = 0.f;
    for (int k = 0; k < 32; ++k) { sg += red[k][0][b][c]; su += red[k][1][b][c]; }
    gbuf[b * FF + blockIdx.x * 32 + c] = rbf(rbf(gelu_tanh(rbf(sg))) * rbf(su));
  }
}

__global__ void __launch_bounds__(256) k_down(const float* __restrict__ hbuf, const float* __restrict__ gbuf,
                                              const bf16_t* __restrict__ wd, bf16_t* __restrict__ out) {
  __shared__ float red[4][16];
  const int tid = threadIdx.x, lane = tid & 63, wave = tid >> 6;
  const int c0 = blockIdx.x * 8;
  float a[16];
#pragma unroll
  for (int i = 0; i < 16; ++i) a[i] = 0.f;
#pragma unroll
  for (int r = 0; r < 7; ++r) {
    const int k = tid + 256 * r;
    const u32x4 w = *(const u32x4*)(wd + (size_t)k * D + c0);
    const float x0 = gbuf[k], x1 = gbuf[FF + k];
#pragma unroll
    for (int j = 0; j < 4; ++j) {
      a[2 * j] = fmaf(x0, bflo(w[j]), a[2 * j]); a[2 * j + 1] = fmaf(x0, bfhi(w[j]), a[2 * j + 1]);
      a[8 + 2 * j] = fmaf(x1, bflo(w[j]), a[8 + 2 * j]); a[8 + 2 * j + 1] = fmaf(x1, bfhi(w[j]), a[8 + 2 * j + 1]);
    }
  }
#pragma unroll
  for (int i = 0; i < 16; ++i) { a[i] = wave_sum(a[i]); }
  if (lane == 0) {
#pragma unroll
    for (int i = 0; i < 16; ++i) red[wave][i] = a[i];
  }
  __syncthreads();
  if (tid < 16) {
    const int b = tid >> 3, c = c0 + (tid & 7);
    const float s = red[0][tid] + red[1][tid] + red[2][tid] + red[3][tid];
    out[b * D + c] = f2bf(hbuf[b * D + c] + rbf(s));
  }
}

extern "C" void kernel_launch(void* const* d_in, const int* in_sizes, int n_in, void* d_out, int out_size, void* d_ws,
                              size_t ws_size, hipStream_t stream) {
  const bf16_t* x = (const bf16_t*)d_in[0];
  const bf16_t* kc = (const bf16_t*)d_in[1];
  const bf16_t* vc = (const bf16_t*)d_in[2];
  const bf16_t* wq = (const bf16_t*)d_in[3];
  const bf16_t* wo = (const bf16_t*)d_in[4];
  const bf16_t* wg = (const bf16_t*)d_in[6];
  const bf16_t* wu = (const bf16_t*)d_in[7];
  const bf16_t* wd = (const bf16_t*)d_in[8];
  char* ws = (char*)d_ws;
  bf16_t* q = (bf16_t*)ws;
  float* part = (float*)(ws + 65536);
  float* apart = (float*)(ws + 65536 + 524288);
  float* hbuf = (float*)(ws + 1048576);
  float* gbuf = (float*)(ws + 1048576 + 65536);
  k_qproj<<<64, 256, 0, stream>>>(x, wq, q);
  k_attn<<<B * H * NSPLIT, NTHR, 0, stream>>>(kc, vc, q, part);
  k_wo<<<64, 256, 0, stream>>>(part, wo, apart);
  k_gateup<<<56, 256, 0, stream>>>(x, apart, wg, wu, hbuf, gbuf);
  k_down<<<56, 256, 0, stream>>>(hbuf, gbuf, wd, (bf16_t*)d_out);
}
```

```cpp
#include <hip/hip_runtime.h>
#include <hip/hip_bf16.h>
#include <stdint.h>

typedef unsigned short bf16_t;
typedef unsigned long long u64;
typedef unsigned u32x4 __attribute__((ext_vector_type(4)));
typedef unsigned u32x2 __attribute__((ext_vector_type(2)));
typedef float f32x4 __attribute__((ext_vector_type(4)));
typedef __bf16 bf16x2 __attribute__((ext_vector_type(2)));
typedef __bf16 bf16x8 __attribute__((ext_vector_type(8)));
#define LAS __attribute__((address_space(3)))
#define GAS __attribute__((address_space(1)))
#define RLX_AGENT __ATOMIC_RELAXED, __HIP_MEMORY_SCOPE_AGENT

constexpr int D = 448, H = 16, SK = 16384, DH = 256, HD = H * DH, FF = 1792;
constexpr int NWG = 256, NTHR = 512;
constexpr int STEP_KEYS = 16, NSTEP = 2048 / STEP_KEYS;
constexpr int NSLOT = 8, SLOT_BYTES = 16384;
#define TW_STEP 32
constexpr int L_FREE = NSLOT * SLOT_BYTES;
constexpr int L_FULL = L_FREE + 16, L_QCNT = L_FREE + 20, L_QRDY = L_FREE + 24;
constexpr int L_QPART = L_FREE + 64;
constexpr int L_Q = L_QPART + 1024;
constexpr int L_MRG = L_Q + 512;
constexpr int L_END = L_MRG + 4 * 260 * 4;
constexpr int LDS_BYTES = L_END;
constexpr int L_RED = 0;
constexpr int L_HN = 8192;
constexpr int L_H = 10240;
constexpr int L_G = 14336;
constexpr int W_GEN = 0;
constexpr int W_FLAG = 4096;
constexpr int W_QG = 8192;
constexpr int W_RECA = W_QG + 65536, RECA = 1152;
constexpr int W_RECB = W_RECA + 256 * RECA;
constexpr int W_RECC = W_RECB + 8 * 2 * D * 4;
constexpr int W_END = W_RECC + 28 * 2 * D * 4;
constexpr int W_STAMP = 1 << 22;
constexpr unsigned SENT = 0xFFFFFFFFu;
constexpr int OOB = 0x7FFFFF00;
constexpr int AUX_SC1 = 16, AUX_NT = 2;
constexpr unsigned SPIN_CAP = 1u << 20;

__device__ __forceinline__ float bflo(unsigned v) { return __builtin_bit_cast(float, v << 16); }
__device__ __forceinline__ float bfhi(unsigned v) { return __builtin_bit_cast(float, v & 0xffff0000u); }
__device__ __forceinline__ unsigned f2bf(float f) { return __builtin_bit_cast(unsigned short, (__bf16)f); }
__device__ __forceinline__ float rbf(float f) { return bflo(f2bf(f)); }
__device__ __forceinline__ unsigned pk2(float lo, float hi) {
  bf16x2 v = {(__bf16)lo, (__bf16)hi};
  return __builtin_bit_cast(unsigned, v);
}
__device__ __forceinline__ float dot2(unsigned k, unsigned q, float acc) {
  return __builtin_amdgcn_fdot2_f32_bf16(__builtin_bit_cast(bf16x2, k), __builtin_bit_cast(bf16x2, q), acc, false);
}
template <int CTRL> __device__ __forceinline__ float dpp(float x) {
  return __builtin_bit_cast(float, __builtin_amdgcn_mov_dpp(__builtin_bit_cast(int, x), CTRL, 0xf, 0xf, true));
}
constexpr int XOR1 = 0xB1, XOR2 = 0x4E, HMIR = 0x141, MIR = 0x140, ROR4 = 0x124, ROR8 = 0x128;
__device__ __forceinline__ float row16_sum(float x) {
  x += dpp<XOR1>(x); x += dpp<XOR2>(x); x += dpp<HMIR>(x); x += dpp<MIR>(x);
  return x;
}
__device__ __forceinline__ float x16_sum(float x) {
  auto s = __builtin_amdgcn_permlane16_swap(__float_as_uint(x), __float_as_uint(x), false, false);
  return __uint_as_float(s[0]) + __uint_as_float(s[1]);
}
__device__ __forceinline__ float x32_sum(float x) {
  auto s = __builtin_amdgcn_permlane32_swap(__float_as_uint(x), __float_as_uint(x), false, false);
  return __uint_as_float(s[0]) + __uint_as_float(s[1]);
}
__device__ __forceinline__ float x16_max(float x) {
  auto s = __builtin_amdgcn_permlane16_swap(__float_as_uint(x), __float_as_uint(x), false, false);
  return fmaxf(__uint_as_float(s[0]), __uint_as_float(s[1]));
}
__device__ __forceinline__ float x32_max(float x) {
  auto s = __builtin_amdgcn_permlane32_swap(__float_as_uint(x), __float_as_uint(x), false, false);
  return fmaxf(__uint_as_float(s[0]), __uint_as_float(s[1]));
}
__device__ __forceinline__ float wave_sum(float x) { return x32_sum(x16_sum(row16_sum(x))); }
__device__ __forceinline__ float tr32(float a, float b) {
  auto s = __builtin_amdgcn_permlane32_swap(__float_as_uint(a), __float_as_uint(b), false, false);
  return __uint_as_float(s[0]) + __uint_as_float(s[1]);
}
__device__ __forceinline__ float tr16(float a, float b) {
  auto s = __builtin_amdgcn_permlane16_swap(__float_as_uint(a), __float_as_uint(b), false, false);
  return __uint_as_float(s[0]) + __uint_as_float(s[1]);
}
__device__ __forceinline__ unsigned canon(float f) {
  const unsigned u = __float_as_uint(f);
  return u == SENT ? 0x7FC00000u : u;
}
__device__ __forceinline__ bool valid4(u32x4 v) { return v[0] != SENT && v[1] != SENT && v[2] != SENT && v[3] != SENT; }
__device__ __forceinline__ f32x4 mfma(u32x4 a, u32x4 b, f32x4 c) {
  return __builtin_amdgcn_mfma_f32_16x16x32_bf16(__builtin_bit_cast(bf16x8, a), __builtin_bit_cast(bf16x8, b), c, 0, 0, 0);
}
template <int N> __device__ __forceinline__ void waitv() { asm volatile("s_waitcnt vmcnt(%0)" ::"i"(N) : "memory"); }
__device__ __forceinline__ void waitl() { asm volatile("s_waitcnt lgkmcnt(0)" ::: "memory"); }
__device__ __forceinline__ void bar() { waitl(); __builtin_amdgcn_s_barrier(); asm volatile("" ::: "memory"); }
__device__ __forceinline__ u64 mix64(u64 z) {
  z += 0x9E3779B97F4A7C15ull; z = (z ^ (z >> 30)) * 0xBF58476D1CE4E5B9ull; z = (z ^ (z >> 27)) * 0x94D049BB133111EBull;
  return z ^ (z >> 31);
}
__device__ __forceinline__ void dma4k(unsigned voff, const char* sb, unsigned lds) {
  asm volatile("s_mov_b32 m0, %2\n\ts_nop 0\n\t"
               "global_load_lds_dwordx4 %0, %1 offset:0 nt\n\t"
               "global_load_lds_dwordx4 %0, %1 offset:1024 nt\n\t"
               "global_load_lds_dwordx4 %0, %1 offset:2048 nt\n\t"
               "global_load_lds_dwordx4 %0, %1 offset:3072 nt"
               ::"v"(voff), "s"(sb), "s"(lds) : "memory");
}
__device__ __forceinline__ void mkfrag(const u32x2 (&raw)[8], u32x4 (&fr)[4]) {
#pragma unroll
  for (int jp = 0; jp < 4; ++jp) {
    fr[0][jp] = __builtin_amdgcn_perm(raw[2 * jp + 1][0], raw[2 * jp][0], 0x05040100u);
    fr[1][jp] = __builtin_amdgcn_perm(raw[2 * jp + 1][0], raw[2 * jp][0], 0x07060302u);
    fr[2][jp] = __builtin_amdgcn_perm(raw[2 * jp + 1][1], raw[2 * jp][1], 0x05040100u);
    fr[3][jp] = __builtin_amdgcn_perm(raw[2 * jp + 1][1], raw[2 * jp][1], 0x07060302u);
  }
}
template <int LD> __device__ __forceinline__ void ldtile(u32x2 (&raw)[8], __amdgpu_buffer_rsrc_t r, int elem0, int lane) {
  const int voff = (8 * (lane >> 4) * LD + 4 * (lane & 15)) * 2;
#pragma unroll
  for (int j = 0; j < 8; ++j) raw[j] = __builtin_amdgcn_raw_buffer_load_b64(r, voff, (elem0 + j * LD) * 2, AUX_NT);
}
__device__ __forceinline__ void mld4(u32x4& v, u64 m, unsigned voff, const unsigned char* sb) {
  asm volatile("s_mov_b64 exec, %1\n\tglobal_load_dwordx4 %0, %2, %3 sc1\n\ts_mov_b64 exec, -1" : "+v"(v) : "s"(m), "v"(voff), "s"(sb) : "memory");
}
__device__ __forceinline__ void mld2(u32x2& v, u64 m, unsigned voff, const unsigned char* sb) {
  asm volatile("s_mov_b64 exec, %1\n\tglobal_load_dwordx2 %0, %2, %3 sc1\n\ts_mov_b64 exec, -1" : "+v"(v) : "s"(m), "v"(voff), "s"(sb) : "memory");
}
__device__ __forceinline__ u64 inval4(u32x4 v) { return __ballot(max(max(v[0], v[1]), max(v[2], v[3])) == SENT); }
__device__ __forceinline__ void pin(u32x4& v) { asm volatile("" : "+v"(v)); }

#define ST(i) do {} while (0)

__global__ void __launch_bounds__(NTHR, 2)
fused_block(const bf16_t* __restrict__ x, const bf16_t* __restrict__ kc, const bf16_t* __restrict__ vc,
            const bf16_t* __restrict__ wq, const bf16_t* __restrict__ wo, const bf16_t* __restrict__ wg,
            const bf16_t* __restrict__ wu, const bf16_t* __restrict__ wd, bf16_t* __restrict__ out, unsigned char* ws) {
  extern __shared__ __attribute__((aligned(16))) unsigned char smem[];
  LAS unsigned char* L = (LAS unsigned char*)smem;
  volatile LAS unsigned* vFULL = (volatile LAS unsigned*)(L + L_FULL);
  volatile LAS unsigned* vFREE = (volatile LAS unsigned*)(L + L_FREE);
  volatile LAS unsigned* vQCNT = (volatile LAS unsigned*)(L + L_QCNT);
  volatile LAS unsigned* vQRDY = (volatile LAS unsigned*)(L + L_QRDY);
  const int tid = threadIdx.x, lane = tid & 63, wave = __builtin_amdgcn_readfirstlane(tid >> 6);
  const int g = blockIdx.x;
  const int bh = g >> 3, sp = g & 7;
  const __amdgpu_buffer_rsrc_t rs = __builtin_amdgcn_make_buffer_rsrc(ws, 0, W_END, 0x00020000);
  const bool roleB = (g & 31) < 7;
  const bool roleC = (g & 63) >= 15 && (g & 63) < 22;
  const int ks = g >> 5, cs = g & 31, jc = (g >> 6) * 7 + (g & 63) - 15;
  if (wave == 0) ST(0);

  const int hq = 2 * (g & 7) + (g >> 7), bq = (g >> 3) & 1, cq = (g >> 4) & 7;
  u32x2 wraw[6][8];
  float m = -INFINITY, l = 0.f, acc[16];
#pragma unroll
  for (int i = 0; i < 16; ++i) acc[i] = 0.f;
  auto load_tail_weights = [&]() {
    if (roleB) {
      const __amdgpu_buffer_rsrc_t r = __builtin_amdgcn_make_buffer_rsrc((void*)wo, 0, HD * D * 2, 0x00020000);
#pragma unroll
      for (int s2 = 0; s2 < 2; ++s2) ldtile<D>(wraw[s2], r, (512 * ks + 64 * wave + 32 * s2) * D + 64 * cs, lane);
    } else if (roleC && wave >= 1) {
      const __amdgpu_buffer_rsrc_t r1 = __builtin_amdgcn_make_buffer_rsrc((void*)wg, 0, FF * D * 2, 0x00020000);
      const __amdgpu_buffer_rsrc_t r2 = __builtin_amdgcn_make_buffer_rsrc((void*)wu, 0, FF * D * 2, 0x00020000);
      const __amdgpu_buffer_rsrc_t r3 = __builtin_amdgcn_make_buffer_rsrc((void*)wd, 0, FF * D * 2, 0x00020000);
#pragma unroll
      for (int s2 = 0; s2 < 2; ++s2) {
        ldtile<FF>(wraw[s2], r1, (64 * (wave - 1) + 32 * s2) * FF + 64 * jc, lane);
        ldtile<FF>(wraw[2 + s2], r2, (64 * (wave - 1) + 32 * s2) * FF + 64 * jc, lane);
        ldtile<D>(wraw[4 + s2], r3, (64 * jc + 32 * s2) * D + 64 * (wave - 1), lane);
      }
    }
    __builtin_amdgcn_sched_barrier(0);
  };

  if (wave == 0) {
    if (lane < 16) ((LAS unsigned*)(L + L_FREE))[lane] = 0u;
    bar();
    const char* kp = (const char*)kc + ((size_t)bh * SK + sp * 2048) * (DH * 2);
    const char* vp = (const char*)vc + ((size_t)bh * SK + sp * 2048) * (DH * 2);
    const unsigned voff = lane * 16;
    unsigned freed = 0, spins = 0;
    auto lstep = [&](int t) {
#pragma unroll 1
      while ((int)freed < t - NSLOT + 1 && spins < SPIN_CAP) {
        const u32x4 f = *(volatile LAS u32x4*)(L + L_FREE);
        freed = __builtin_amdgcn_readfirstlane(min(min(f[0], f[1]), min(f[2], f[3])));
        if ((int)freed < t - NSLOT + 1) { __builtin_amdgcn_s_sleep(1); ++spins; }
      }
      const unsigned lb = (t & (NSLOT - 1)) * SLOT_BYTES;
      dma4k(voff, kp, lb); dma4k(voff, kp + 4096, lb + 4096);
      dma4k(voff, vp, lb + 8192); dma4k(voff, vp + 4096, lb + 12288);
      kp += 8192; vp += 8192;
      waitv<16>();
      *vFULL = t;
    };
#pragma unroll 1
    for (int t = 0; t < TW_STEP; ++t) lstep(t);
    load_tail_weights();
#pragma unroll 1
    for (int t = TW_STEP; t < NSTEP; ++t) lstep(t);
    waitv<0>();
    *vFULL = NSTEP;
    ST(3);
  } else {
    u64 gen;
    {
      u32x4 wqr[4], xall;
      unsigned xr[4];
      const int row0 = 64 * (wave - 1) + (lane >> 2);
      xall = *(const GAS u32x4*)(x + bq * D + 8 * min(lane, 55));
#pragma unroll
      for (int i = 0; i < 4; ++i) xr[i] = *(const GAS unsigned short*)(x + bq * D + row0 + 16 * i);
#pragma unroll
      for (int i = 0; i < 4; ++i)
        wqr[i] = __builtin_nontemporal_load((const GAS u32x4*)(wq + (size_t)(row0 + 16 * i) * HD + hq * DH + 32 * cq + 8 * (lane & 3)));
      unsigned z = 0;
      asm volatile("" : "+v"(z));
      gen = __hip_atomic_load((GAS u64*)(ws + W_GEN + z), RLX_AGENT);
      __builtin_amdgcn_sched_barrier(0);
      bar();
      __builtin_amdgcn_sched_barrier(0);
      if (lane >= 56) xall = u32x4{0u, 0u, 0u, 0u};
      float xs[8]; float s = 0.f;
#pragma unroll
      for (int i = 0; i < 4; ++i) { xs[2 * i] = bflo(xall[i]); xs[2 * i + 1] = bfhi(xall[i]); s += xs[2 * i] + xs[2 * i + 1]; }
      const float mean = wave_sum(s) * (1.f / D);
      float qv = 0.f;
#pragma unroll
      for (int i = 0; i < 8; ++i) { const float d = xs[i] - mean; qv = fmaf(d, d, qv); }
      if (lane >= 56) qv = 0.f;
      const float var = wave_sum(qv) * (1.f / D);
      const float mb = rbf(mean);
      const float rsd = __builtin_amdgcn_rcpf(rbf(__builtin_amdgcn_sqrtf(rbf(rbf(var) + 1e-5f))));
      float a[8];
#pragma unroll
      for (int j = 0; j < 8; ++j) a[j] = 0.f;
#pragma unroll
      for (int i = 0; i < 4; ++i) {
        const float xn = rbf(rbf(bflo(xr[i]) - mb) * rsd);
#pragma unroll
        for (int j = 0; j < 4; ++j) { a[2 * j] = fmaf(xn, bflo(wqr[i][j]), a[2 * j]); a[2 * j + 1] = fmaf(xn, bfhi(wqr[i][j]), a[2 * j + 1]); }
      }
      float b4[4], b2[2];
#pragma unroll
      for (int j = 0; j < 4; ++j) b4[j] = tr32(a[j], a[j + 4]);
#pragma unroll
      for (int j = 0; j < 2; ++j) b2[j] = tr16(b4[j], b4[j + 2]);
#pragma unroll
      for (int j = 0; j < 2; ++j) { b2[j] += dpp<ROR4>(b2[j]); b2[j] += dpp<ROR8>(b2[j]); }
      if ((lane & 12) == 0) {
        const int col = 8 * (lane & 3) + 4 * (lane >> 5) + 2 * ((lane >> 4) & 1);
        LAS float* qp = (LAS float*)(L + L_QPART) + (wave - 1) * 32 + col;
        qp[0] = b2[0]; qp[1] = b2[1];
      }
      waitl();
      if (lane == 0) __hip_atomic_fetch_add((LAS unsigned*)(L + L_QCNT), 1u, __ATOMIC_RELAXED, __HIP_MEMORY_SCOPE_WORKGROUP);
    }
    const u64 tag = mix64(((u64)__builtin_amdgcn_readfirstlane((unsigned)(gen >> 32)) << 32) | __builtin_amdgcn_readfirstlane((unsigned)gen));
    if (wave == 1) {
      unsigned spins = 0;
#pragma unroll 1
      while (__builtin_amdgcn_readfirstlane(*vQCNT) < 7u && ++spins < SPIN_CAP) {}
      asm volatile("" ::: "memory");
      ST(1);
      const u64 t48 = tag & ~0xFFFFull;
      if (lane < 32) {
        float s = 0.f;
#pragma unroll
        for (int w = 0; w < 7; ++w) s += ((LAS float*)(L + L_QPART))[w * 32 + lane];
        __hip_atomic_store((GAS u64*)(ws + W_QG) + bq * HD + hq * DH + 32 * cq + lane, t48 | f2bf(s), RLX_AGENT);
      }
      GAS u64* qg = (GAS u64*)(ws + W_QG) + (bh >> 4) * HD + (bh & 15) * DH + lane;
      u64 v[4];
#pragma unroll 1
      for (spins = 0; spins < SPIN_CAP; ++spins) {
        bool ok = true;
#pragma unroll
        for (int i = 0; i < 4; ++i) v[i] = __hip_atomic_load(qg + 64 * i, RLX_AGENT);
#pragma unroll
        for (int i = 0; i < 4; ++i) ok &= (v[i] & ~0xFFFFull) == t48;
        if (__all(ok)) break;
      }
#pragma unroll
      for (int i = 0; i < 4; ++i) ((LAS unsigned short*)(L + L_Q))[64 * i + lane] = (unsigned short)v[i];
      waitl();
      *vQRDY = 1u;
      ST(2);
    } else if (wave == 2) {
      const u32x4 sv = {SENT, SENT, SENT, SENT};
      __builtin_amdgcn_raw_buffer_store_b128(sv, rs, W_RECA + g * RECA + 16 * lane, 0, AUX_SC1);
      __builtin_amdgcn_raw_buffer_store_b128(sv, rs, lane == 0 ? W_RECA + g * RECA + 1024 : OOB, 0, AUX_SC1);
      if (roleB) __builtin_amdgcn_raw_buffer_store_b128(sv, rs, lane < 32 ? W_RECB + ((ks * 2 + (lane >> 4)) * D + 64 * cs + 4 * (lane & 15)) * 4 : OOB, 0, AUX_SC1);
      if (roleC) {
#pragma unroll
        for (int i = 0; i < 4; ++i) __builtin_amdgcn_raw_buffer_store_b128(sv, rs, lane < 56 ? W_RECC + jc * (2 * D * 4) + (56 * i + lane) * 16 : OOB, 0, AUX_SC1);
      }
      waitv<0>();
      if (lane == 0) __hip_atomic_store((GAS u64*)(ws + W_FLAG) + g, tag, RLX_AGENT);
      if (roleB || roleC) {
        unsigned spins = 0;
#pragma unroll 1
        while (__builtin_amdgcn_readfirstlane(*vFULL) < 16u && ++spins < SPIN_CAP) __builtin_amdgcn_s_sleep(8);
#pragma unroll 1
        for (spins = 0; spins < SPIN_CAP; ++spins) {
          bool ok = true;
#pragma unroll
          for (int i = 0; i < 4; ++i) ok &= __hip_atomic_load((GAS u64*)(ws + W_FLAG) + 64 * i + lane, RLX_AGENT) == tag;
          if (__all(ok)) break;
          __builtin_amdgcn_s_sleep(8);
        }
        if (g == 0 && lane == 0) __hip_atomic_store((GAS u64*)(ws + W_GEN), tag, RLX_AGENT);
      }
    }
    if (wave < 4) {
      unsigned spins = 0;
#pragma unroll 1
      while (__builtin_amdgcn_readfirstlane(*vFULL) < (unsigned)TW_STEP && ++spins < SPIN_CAP) __builtin_amdgcn_s_sleep(8);
      load_tail_weights();
    } else {
      const int ci = wave - 4, r = lane >> 4, c = lane & 15;
      unsigned spins = 0;
#pragma unroll 1
      while (__builtin_amdgcn_readfirstlane(*vQRDY) == 0u && ++spins < SPIN_CAP) __builtin_amdgcn_s_sleep(1);
      asm volatile("" ::: "memory");
      const u32x4 q0 = *(LAS u32x4*)(L + L_Q + 16 * c), q1 = *(LAS u32x4*)(L + L_Q + 256 + 16 * c);
      constexpr float C2 = 0.0625f * 1.4426950408889634f;
      const int lo = (4 * ci + r) * 512 + 16 * c;
      unsigned full = 0;
      auto cstep = [&](int t) {
#pragma unroll 1
        while ((int)full <= t && spins < SPIN_CAP) { full = __builtin_amdgcn_readfirstlane(*vFULL); ++spins; }
        asm volatile("" ::: "memory");
        LAS unsigned char* sb = L + (t & (NSLOT - 1)) * SLOT_BYTES + lo;
        u32x4 k0 = *(LAS u32x4*)(sb), k1 = *(LAS u32x4*)(sb + 256), v0 = *(LAS u32x4*)(sb + 8192), v1 = *(LAS u32x4*)(sb + 8192 + 256);
        waitl();
        vFREE[ci] = t + 1;
        asm volatile("" : "+v"(k0), "+v"(k1), "+v"(v0), "+v"(v1)::"memory");
        float s0 = 0.f, s1 = 0.f;
#pragma unroll
        for (int j = 0; j < 4; ++j) { s0 = dot2(k0[j], q0[j], s0); s1 = dot2(k1[j], q1[j], s1); }
        float s = rbf(row16_sum(s0 + s1)) * C2;
        const float mn = fmaxf(m, s);
        const float al = __builtin_amdgcn_exp2f(m - mn), p = __builtin_amdgcn_exp2f(s - mn);
        l = fmaf(l, al, p); m = mn;
#pragma unroll
        for (int j = 0; j < 4; ++j) {
          acc[2 * j] = fmaf(p, bflo(v0[j]), acc[2 * j] * al);
          acc[2 * j + 1] = fmaf(p, bfhi(v0[j]), acc[2 * j + 1] * al);
          acc[8 + 2 * j] = fmaf(p, bflo(v1[j]), acc[8 + 2 * j] * al);
          acc[8 + 2 * j + 1] = fmaf(p, bfhi(v1[j]), acc[8 + 2 * j + 1] * al);
        }
      };
#pragma unroll 1
      for (int t = 0; t < TW_STEP; ++t) cstep(t);
      load_tail_weights();
#pragma unroll 1
      for (int t = TW_STEP; t < NSTEP; ++t) cstep(t);
      if (wave == 4) ST(4);
      const float M = x32_max(x16_max(m));
      const float sc = __builtin_amdgcn_exp2f(m - M);
      const float Lw = x32_sum(x16_sum(l * sc));
      float a8[8], a4[4];
#pragma unroll
      for (int j = 0; j < 8; ++j) a8[j] = tr32(acc[j] * sc, acc[j + 8] * sc);
#pragma unroll
      for (int j = 0; j < 4; ++j) a4[j] = tr16(a8[j], a8[j + 4]);
      LAS float* rec = (LAS float*)(L + L_MRG) + ci * 260;
      *(LAS f32x4*)(rec + (r >> 1) * 128 + 8 * c + (r & 1) * 4) = f32x4{a4[0], a4[1], a4[2], a4[3]};
      if (lane == 0) { rec[256] = M; rec[257] = Lw; }
    }
  }
  bar();
  if (wave == 4) {
    const LAS float* mg = (const LAS float*)(L + L_MRG);
    float Mi[4], M = -INFINITY;
#pragma unroll
    for (int i = 0; i < 4; ++i) { Mi[i] = mg[i * 260 + 256]; M = fmaxf(M, Mi[i]); }
    float Ls = 0.f; f32x4 o = {0.f, 0.f, 0.f, 0.f};
#pragma unroll
    for (int i = 0; i < 4; ++i) {
      const float w = __builtin_amdgcn_exp2f(Mi[i] - M);
      Ls = fmaf(w, mg[i * 260 + 257], Ls);
      o += w * *(const LAS f32x4*)(mg + i * 260 + 4 * lane);
    }
    __builtin_amdgcn_raw_buffer_store_b128(u32x4{canon(o[0]), canon(o[1]), canon(o[2]), canon(o[3])}, rs, W_RECA + g * RECA + 16 * lane, 0, AUX_SC1);
    __builtin_amdgcn_raw_buffer_store_b64(u32x2{canon(M), canon(Ls)}, rs, lane == 0 ? W_RECA + g * RECA + 1024 : OOB, 0, AUX_SC1);
    ST(5);
  }
  const int n = lane & 15, kg = lane >> 4;
  if (roleB) {
    u32x4 fr[2][4];
    mkfrag(wraw[0], fr[0]); mkfrag(wraw[1], fr[1]);
    const int rec = W_RECA + (((n & 1) * 16 + 2 * ks + (wave >> 2)) * 8 + (n >> 1)) * RECA;
    const int off = rec + (64 * (wave & 3) + 8 * kg) * 4;
    u32x4 v[4]; u32x2 hd = {SENT, SENT};
#pragma unroll
    for (int i = 0; i < 4; ++i) v[i] = u32x4{SENT, SENT, SENT, SENT};
#pragma unroll 1
    for (unsigned spins = 0; spins < SPIN_CAP; ++spins) {
      u64 mk[5], any;
#pragma unroll
      for (int i = 0; i < 4; ++i) mk[i] = inval4(v[i]);
      mk[4] = __ballot(max(hd[0], hd[1]) == SENT);
      any = (mk[0] | mk[1]) | (mk[2] | mk[3]) | mk[4];
      if (any == 0) break;
#pragma unroll
      for (int i = 0; i < 4; ++i) mld4(v[i], mk[i], off, ws + (i >> 1) * 128 + (i & 1) * 16);
      mld2(hd, mk[4], rec, ws + 1024);
      asm volatile("s_waitcnt vmcnt(0)" : "+v"(v[0]), "+v"(v[1]), "+v"(v[2]), "+v"(v[3]), "+v"(hd)::"memory");
    }
    if (wave == 0) ST(6);
    const float mi = __uint_as_float(hd[0]);
    float M = fmaxf(mi, dpp<XOR2>(mi)); M = fmaxf(M, dpp<ROR4>(M)); M = fmaxf(M, dpp<ROR8>(M));
    const float w = __builtin_amdgcn_exp2f(mi - M);
    float Ls = w * __uint_as_float(hd[1]);
    Ls += dpp<XOR2>(Ls); Ls += dpp<ROR4>(Ls); Ls += dpp<ROR8>(Ls);
    const float sc = w * __builtin_amdgcn_rcpf(Ls);
    u32x4 af[2];
#pragma unroll
    for (int i = 0; i < 4; ++i) {
      float e[4];
#pragma unroll
      for (int j = 0; j < 4; ++j) {
        float t = __uint_as_float(v[i][j]) * sc;
        t += dpp<XOR2>(t); t += dpp<ROR4>(t); t += dpp<ROR8>(t);
        e[j] = t;
      }
      af[i >> 1][(i & 1) * 2] = pk2(e[0], e[1]); af[i >> 1][(i & 1) * 2 + 1] = pk2(e[2], e[3]);
    }
    f32x4 d[4];
#pragma unroll
    for (int c = 0; c < 4; ++c) { d[c] = mfma(af[0], fr[0][c], f32x4{0.f, 0.f, 0.f, 0.f}); d[c] = mfma(af[1], fr[1][c], d[c]); }
    if (kg < 2) {
      const f32x4 o = kg == 0 ? f32x4{d[0][0], d[1][0], d[2][0], d[3][0]} : f32x4{d[0][1], d[1][1], d[2][1], d[3][1]};
      *(LAS f32x4*)(L + L_RED + ((wave * 2 + kg) * 64 + 4 * n) * 4) = o;
    }
    bar();
    if (wave == 0) {
      f32x4 s = {0.f, 0.f, 0.f, 0.f};
#pragma unroll
      for (int w2 = 0; w2 < 8; ++w2) s += *(LAS f32x4*)(L + L_RED + ((w2 * 2 + (kg & 1)) * 64 + 4 * n) * 4);
      __builtin_amdgcn_raw_buffer_store_b128(u32x4{canon(s[0]), canon(s[1]), canon(s[2]), canon(s[3])}, rs,
                                             lane < 32 ? W_RECB + ((ks * 2 + kg) * D + 64 * cs + 4 * n) * 4 : OOB, 0, AUX_SC1);
      ST(7);
    }
  } else if (roleC) {
    u32x4 fr[6][4];
    if (wave >= 1) {
#pragma unroll
      for (int i = 0; i < 6; ++i) mkfrag(wraw[i], fr[i]);
    }
    if (wave == 1 || wave == 2) {
      const int b = wave - 1;
      u32x4 xv = {0u, 0u, 0u, 0u};
      if (lane < 56) xv = *(const GAS u32x4*)(x + b * D + 8 * lane);
      const int off = W_RECB + (b * D + 8 * lane) * 4;
      u32x4 v[16];
      {
        const unsigned iv = lane < 56 ? SENT : 0u;
#pragma unroll
        for (int i = 0; i < 16; ++i) v[i] = u32x4{iv, iv, iv, iv};
      }
#pragma unroll 1
    for (unsigned spins = 0; spins < SPIN_CAP; ++spins) {
        u64 mk[16], any = 0;
#pragma unroll
        for (int i = 0; i < 16; ++i) { mk[i] = inval4(v[i]); any |= mk[i]; }
        if (any == 0) break;
#pragma unroll
        for (int i = 0; i < 16; ++i) mld4(v[i], mk[i], off, ws + (i >> 1) * (2 * D * 4) + (i & 1) * 16);
        waitv<0>();
#pragma unroll
        for (int i = 0; i < 16; ++i) pin(v[i]);
      }
      if (wave == 1) ST(8);
      float hv[8]; float s = 0.f;
#pragma unroll
      for (int j = 0; j < 8; ++j) {
        float a = 0.f;
#pragma unroll
        for (int k = 0; k < 8; ++k) a += __uint_as_float(v[2 * k + (j >> 2)][j & 3]);
        const float xx = (j & 1) ? bfhi(xv[j >> 1]) : bflo(xv[j >> 1]);
        hv[j] = rbf(xx + rbf(a));
        s += hv[j];
      }
      const float mean = wave_sum(s) * (1.f / D);
      float qv = 0.f;
#pragma unroll
      for (int j = 0; j < 8; ++j) { const float dd = hv[j] - mean; qv = fmaf(dd, dd, qv); }
      if (lane >= 56) qv = 0.f;
      const float var = wave_sum(qv) * (1.f / D);
      const float mb = rbf(mean);
      const float rsd = __builtin_amdgcn_rcpf(rbf(__builtin_amdgcn_sqrtf(rbf(rbf(var) + 1e-5f))));
      if (lane < 56) {
        u32x4 hn;
#pragma unroll
        for (int j = 0; j < 4; ++j) hn[j] = pk2(rbf(hv[2 * j] - mb) * rsd, rbf(hv[2 * j + 1] - mb) * rsd);
        *(LAS u32x4*)(L + L_HN + (b * D + 8 * lane) * 2) = hn;
        *(LAS f32x4*)(L + L_H + (b * D + 8 * lane) * 4) = f32x4{hv[0], hv[1], hv[2], hv[3]};
        *(LAS f32x4*)(L + L_H + (b * D + 8 * lane) * 4 + 16) = f32x4{hv[4], hv[5], hv[6], hv[7]};
      }
    }
    bar();
    if (wave >= 1) {
      const LAS unsigned char* ap = L + L_HN + ((n & 1) * D + 64 * (wave - 1) + 8 * kg) * 2;
      const u32x4 a0 = *(const LAS u32x4*)ap, a1 = *(const LAS u32x4*)(ap + 64);
      f32x4 dg[4], du[4];
#pragma unroll
      for (int c = 0; c < 4; ++c) {
        dg[c] = mfma(a0, fr[0][c], f32x4{0.f, 0.f, 0.f, 0.f}); du[c] = mfma(a0, fr[2][c], f32x4{0.f, 0.f, 0.f, 0.f});
        dg[c] = mfma(a1, fr[1][c], dg[c]); du[c] = mfma(a1, fr[3][c], du[c]);
      }
      f32x4 o;
#pragma unroll
      for (int c = 0; c < 4; ++c) {
        const float t0 = (lane & 16) ? dg[c][1] : dg[c][0], t1 = (lane & 16) ? du[c][1] : du[c][0];
        o[c] = (lane & 32) ? t1 : t0;
      }
      *(LAS f32x4*)(L + L_RED + (((wave - 1) * 4 + kg) * 64 + 4 * n) * 4) = o;
    }
    bar();
    if (wave == 1 || wave == 2) {
      const int b = wave - 1;
      float sg = 0.f, su = 0.f;
#pragma unroll
      for (int w2 = 0; w2 < 7; ++w2) {
        sg += *(LAS float*)(L + L_RED + ((w2 * 4 + b) * 64 + lane) * 4);
        su += *(LAS float*)(L + L_RED + ((w2 * 4 + 2 + b) * 64 + lane) * 4);
      }
      const float xg = rbf(sg);
      const float u2 = (2.f * 0.7978845608028654f * 1.4426950408889634f) * (xg + 0.044715f * xg * xg * xg);
      const float ge = xg * __builtin_amdgcn_rcpf(1.f + __builtin_amdgcn_exp2f(-u2));
      *(LAS unsigned short*)(L + L_G + (b * 64 + lane) * 2) = (unsigned short)f2bf(rbf(ge) * rbf(su));
    }
    bar();
    if (wave >= 1) {
      const LAS unsigned char* ap = L + L_G + ((n & 1) * 64 + 8 * kg) * 2;
      const u32x4 a0 = *(const LAS u32x4*)ap, a1 = *(const LAS u32x4*)(ap + 64);
      f32x4 d[4];
#pragma unroll
      for (int c = 0; c < 4; ++c) { d[c] = mfma(a0, fr[4][c], f32x4{0.f, 0.f, 0.f, 0.f}); d[c] = mfma(a1, fr[5][c], d[c]); }
      const f32x4 o = kg == 0 ? f32x4{d[0][0], d[1][0], d[2][0], d[3][0]} : f32x4{d[0][1], d[1][1], d[2][1], d[3][1]};
      __builtin_amdgcn_raw_buffer_store_b128(u32x4{canon(o[0]), canon(o[1]), canon(o[2]), canon(o[3])}, rs,
                                             lane < 32 ? W_RECC + ((jc * 2 + kg) * D + 64 * (wave - 1) + 4 * n) * 4 : OOB, 0, AUX_SC1);
      if (wave == 1) ST(9);
    } else {
      const int pg = lane >> 3, ch = lane & 7;
      const int off = W_RECC + pg * (2 * D * 4) + (32 * jc + 4 * ch) * 4;
      u32x4 v[4];
#pragma unroll
      for (int i = 0; i < 4; ++i) { const unsigned iv = (i < 3 || pg < 4) ? SENT : 0u; v[i] = u32x4{iv, iv, iv, iv}; }
#pragma unroll 1
    for (unsigned spins = 0; spins < SPIN_CAP; ++spins) {
        u64 mk[4], any = 0;
#pragma unroll
        for (int i = 0; i < 4; ++i) { mk[i] = inval4(v[i]); any |= mk[i]; }
        if (any == 0) break;
#pragma unroll
        for (int i = 0; i < 4; ++i) mld4(v[i], mk[i], off, ws + i * 8 * (2 * D * 4));
        asm volatile("s_waitcnt vmcnt(0)" : "+v"(v[0]), "+v"(v[1]), "+v"(v[2]), "+v"(v[3])::"memory");
      }
      ST(10);
      float s[4];
#pragma unroll
      for (int j = 0; j < 4; ++j) {
        float t = (__uint_as_float(v[0][j]) + __uint_as_float(v[1][j])) + (__uint_as_float(v[2][j]) + __uint_as_float(v[3][j]));
        t += dpp<ROR8>(t);
        s[j] = x32_sum(x16_sum(t));
      }
      if (lane < 8) {
        const f32x4 hh = *(LAS f32x4*)(L + L_H + (32 * jc + 4 * ch) * 4);
        const u32x2 o = {pk2(hh[0] + rbf(s[0]), hh[1] + rbf(s[1])), pk2(hh[2] + rbf(s[2]), hh[3] + rbf(s[3]))};
        *(GAS u32x2*)(out + 32 * jc + 4 * ch) = o;
      }
      ST(11);
    }
  }
}

extern "C" void kernel_launch(void* const* d_in, const int* in_sizes, int n_in, void* d_out, int out_size, void* d_ws,
                              size_t ws_size, hipStream_t stream) {
  (void)hipFuncSetAttribute((const void*)fused_block, hipFuncAttributeMaxDynamicSharedMemorySize, LDS_BYTES);
  hipLaunchKernelGGL(fused_block, dim3(NWG), dim3(NTHR), LDS_BYTES, stream, (const bf16_t*)d_in[0], (const bf16_t*)d_in[1],
                     (const bf16_t*)d_in[2], (const bf16_t*)d_in[3], (const bf16_t*)d_in[4], (const bf16_t*)d_in[6],
                     (const bf16_t*)d_in[7], (const bf16_t*)d_in[8], (bf16_t*)d_out, (unsigned char*)d_ws);
}
```

```cpp
#include <hip/hip_runtime.h>
#include <hip/hip_bf16.h>
#include <stdint.h>
#include <type_traits>

typedef unsigned short bf16_t;
typedef unsigned long long u64;
typedef unsigned u32x4 __attribute__((ext_vector_type(4)));
typedef unsigned u32x2 __attribute__((ext_vector_type(2)));
typedef float f32x4 __attribute__((ext_vector_type(4)));
typedef float f32x2 __attribute__((ext_vector_type(2)));
typedef __bf16 bf16x2 __attribute__((ext_vector_type(2)));
typedef __bf16 bf16x8 __attribute__((ext_vector_type(8)));
#define LAS __attribute__((address_space(3)))
#define GAS __attribute__((address_space(1)))
#define RLX_AGENT __ATOMIC_RELAXED, __HIP_MEMORY_SCOPE_AGENT
#define RLX_WG __ATOMIC_RELAXED, __HIP_MEMORY_SCOPE_WORKGROUP

constexpr int D = 448, SK = 16384, DH = 256, HD = 4096, FF = 1792;
constexpr int NWG = 256, NTHR = 768;
constexpr int NPH = 64, NSTEP = 2 * NPH;
constexpr int NSLOT = 8, SLOT_BYTES = 16384;
#define W0 20
#define WMIN 17
#define WMAX 32
#define TSCHED 7000
#define LEAD 200
#define NRAMP 8
#define WSTART 28
#define ADVB 0
#define ADVC 0
#define GAIN 8
#define PRE 8
#define TWB 24
#define TWC 24
#define TWS 6
#define ML_STEP 96
#define LPRIO 3
#define CSLEEP 1
#define DRYRUN 0
constexpr int L_CTL = NSLOT * SLOT_BYTES;
constexpr int L_FREE = L_CTL, L_QCNT = L_CTL + 20, L_QRDY = L_CTL + 24, L_C1 = L_CTL + 28, L_C2 = L_CTL + 32,
              L_SCRDY = L_CTL + 36, L_SCALE = L_CTL + 40, L_MW = L_CTL + 64, L_LW = L_CTL + 80;
constexpr int L_FULL = L_CTL + 128;
constexpr int L_QPART = L_FULL + 256;
constexpr int L_Q = L_QPART + 1024;
constexpr int L_P = L_Q + 512;
constexpr int L_RED = L_P + 8192;
constexpr int L_ST = L_RED + 10752;
constexpr int L_H = L_ST + 256;
constexpr int L_G = L_H + 1792;
constexpr int L_DBG = L_G + 256;
constexpr int LDS_BYTES = L_DBG + 256;
static_assert(LDS_BYTES <= 160 * 1024, "LDS");
constexpr int W_GEN = 0;
constexpr int W_FLAG = 4096;
constexpr int W_ML = 8192;
constexpr int W_QG = 12288;
constexpr int W_RECA = W_QG + 65536, RECA = 512;
constexpr int W_RECB = W_RECA + 256 * RECA;
constexpr int W_RECC = W_RECB + 8 * 2 * D * 4;
constexpr int W_END = W_RECC + 28 * 2 * D * 4;
constexpr int W_STAMP = 1 << 22;
constexpr unsigned SENT = 0xFFFFFFFFu;
constexpr int OOB = 0x40000000;
constexpr int AUX_SC1 = 16, AUX_NT = 2;
constexpr unsigned SPIN_CAP = 1u << 20;

__device__ __forceinline__ float bflo(unsigned v) { return __builtin_bit_cast(float, v << 16); }
__device__ __forceinline__ float bfhi(unsigned v) { return __builtin_bit_cast(float, v & 0xffff0000u); }
__device__ __forceinline__ unsigned f2bf(float f) { return __builtin_bit_cast(unsigned short, (__bf16)f); }
__device__ __forceinline__ float rbf(float f) { return bflo(f2bf(f)); }
__device__ __forceinline__ unsigned pk2(float lo, float hi) {
  bf16x2 v = {(__bf16)lo, (__bf16)hi};
  return __builtin_bit_cast(unsigned, v);
}
__device__ __forceinline__ float dot2(unsigned k, unsigned q, float acc) {
  return __builtin_amdgcn_fdot2_f32_bf16(__builtin_bit_cast(bf16x2, k), __builtin_bit_cast(bf16x2, q), acc, false);
}
template <int CTRL> __device__ __forceinline__ unsigned dppu(unsigned x) { return __builtin_amdgcn_mov_dpp((int)x, CTRL, 0xf, 0xf, true); }
template <int CTRL> __device__ __forceinline__ float dpp(float x) { return __uint_as_float(dppu<CTRL>(__float_as_uint(x))); }
constexpr int XOR1 = 0xB1, XOR2 = 0x4E, HMIR = 0x141, MIR = 0x140, ROR4 = 0x124, ROR8 = 0x128, SHL = 0x100;
__device__ __forceinline__ float row16_sum(float x) {
  x += dpp<XOR1>(x); x += dpp<XOR2>(x); x += dpp<HMIR>(x); x += dpp<MIR>(x);
  return x;
}
__device__ __forceinline__ float tr32(float a, float b) {
  auto s = __builtin_amdgcn_permlane32_swap(__float_as_uint(a), __float_as_uint(b), false, false);
  return __uint_as_float(s[0]) + __uint_as_float(s[1]);
}
__device__ __forceinline__ float tr16(float a, float b) {
  auto s = __builtin_amdgcn_permlane16_swap(__float_as_uint(a), __float_as_uint(b), false, false);
  return __uint_as_float(s[0]) + __uint_as_float(s[1]);
}
__device__ __forceinline__ float x16_sum(float x) { return tr16(x, x); }
__device__ __forceinline__ float x32_sum(float x) { return tr32(x, x); }
__device__ __forceinline__ float x16_max(float x) {
  auto s = __builtin_amdgcn_permlane16_swap(__float_as_uint(x), __float_as_uint(x), false, false);
  return fmaxf(__uint_as_float(s[0]), __uint_as_float(s[1]));
}
__device__ __forceinline__ float x32_max(float x) {
  auto s = __builtin_amdgcn_permlane32_swap(__float_as_uint(x), __float_as_uint(x), false, false);
  return fmaxf(__uint_as_float(s[0]), __uint_as_float(s[1]));
}
__device__ __forceinline__ float wave_sum(float x) { return x32_sum(x16_sum(row16_sum(x))); }
__device__ __forceinline__ unsigned canonu(unsigned u) { return u == SENT ? 0x7FC07FC0u : u; }
__device__ __forceinline__ unsigned canon(float f) { return canonu(__float_as_uint(f)); }
__device__ __forceinline__ f32x4 mfma(u32x4 a, u32x4 b, f32x4 c) {
  return __builtin_amdgcn_mfma_f32_16x16x32_bf16(__builtin_bit_cast(bf16x8, a), __builtin_bit_cast(bf16x8, b), c, 0, 0, 0);
}
template <int N> __device__ __forceinline__ void waitv() { asm volatile("s_waitcnt vmcnt(%0)" ::"i"(N) : "memory"); }
__device__ __forceinline__ void waitl() { asm volatile("s_waitcnt lgkmcnt(0)" ::: "memory"); }
__device__ __forceinline__ void bar() { waitl(); __builtin_amdgcn_s_barrier(); asm volatile("" ::: "memory"); }
__device__ __forceinline__ u64 mix64(u64 z) {
  z += 0x9E3779B97F4A7C15ull; z = (z ^ (z >> 30)) * 0xBF58476D1CE4E5B9ull; z = (z ^ (z >> 27)) * 0x94D049BB133111EBull;
  return z ^ (z >> 31);
}
#define STR2(x) #x
#define STR(x) STR2(x)
#define NTS " nt"
#define BIGINC "0xe800"
#define BIGHI ""
#define DMA4(a, b, c, d) \
  "global_load_lds_dwordx4 %[" #a "], s[60:61] offset:0" NTS "\n\t" \
  "global_load_lds_dwordx4 %[" #b "], s[60:61] offset:1024" NTS "\n\t" \
  "global_load_lds_dwordx4 %[" #c "], s[60:61] offset:2048" NTS "\n\t" \
  "global_load_lds_dwordx4 %[" #d "], s[60:61] offset:3072" NTS "\n\t"
__device__ __forceinline__ void stream_steps(const unsigned (&v)[16], const char*& p, int& t, int tend, int& w, int& pub, int& sch,
                                             int slope, unsigned t0, unsigned a_free, unsigned a_full) {
  unsigned vf, vt;
  asm volatile(
      "s_mov_b64 s[60:61], %[p]\n\t"
      "s_getpc_b64 s[62:63]\n"
      "0:\n\t"
      "s_add_u32 s62, s62, 9f-0b-8*" STR(WMIN) "\n\t"
      "s_addc_u32 s63, s63, 0\n\t"
      "s_mov_b32 s73, 0x100000\n"
      "1:\n\t"
      "s_memrealtime s[64:65]\n\t"
      "ds_read_b32 %[vf], %[af]\n\t"
      "s_lshl_b32 s66, %[w], 3\n\t"
      "s_add_u32 s70, s62, s66\n\t"
      "s_addc_u32 s71, s63, 0\n\t"
      "s_lshl_b32 s66, %[t], 14\n\t"
      "s_and_b32 m0, s66, 0x1c000\n\t"
      "s_swappc_b64 s[68:69], s[70:71]\n\t"
      DMA4(v0, v1, v2, v3)
      "s_add_u32 m0, m0, 0x1000\n\t"
      "s_swappc_b64 s[68:69], s[70:71]\n\t"
      DMA4(v4, v5, v6, v7)
      "s_add_u32 m0, m0, 0x1000\n\t"
      "s_swappc_b64 s[68:69], s[70:71]\n\t"
      DMA4(v8, v9, v10, v11)
      "s_add_u32 m0, m0, 0x1000\n\t"
      "s_swappc_b64 s[68:69], s[70:71]\n\t"
      DMA4(v12, v13, v14, v15)
      "s_lshl_b32 s66, %[t], 4\n\t"
      "s_add_i32 s66, s66, 12\n\t"
      "s_sub_i32 s66, s66, %[w]\n\t"
      "s_ashr_i32 s66, s66, 4\n\t"
      "s_max_i32 %[pub], %[pub], s66\n\t"
      "v_mov_b32 %[vt], %[pub]\n\t"
      "s_waitcnt lgkmcnt(0)\n\t"
      "ds_write_b32 %[al], %[vt]\n\t"
      "s_sub_u32 s64, s64, %[t0]\n\t"
      "s_lshl_b32 s64, s64, 7\n\t"
      "s_sub_i32 s64, s64, %[sch]\n\t"
      "s_add_i32 %[sch], %[sch], %[slope]\n\t"
      "s_mulk_i32 s64, " STR(GAIN) "\n\t"
      "s_ashr_i32 s64, s64, 15\n\t"
      "s_add_i32 s64, s64, " STR(W0) "\n\t"
      "s_max_i32 s64, s64, " STR(WMIN) "\n\t"
      "s_min_i32 %[w], s64, " STR(WMAX) "\n\t"
      "s_and_b32 s66, %[t], 3\n\t"
      "s_movk_i32 s67, 0x800\n\t"
      "s_mov_b32 s72, 0\n\t"
      "s_cmp_eq_u32 s66, 3\n\t"
      "s_cmov_b32 s67, " BIGINC "\n\t"
      BIGHI
      "s_add_u32 s60, s60, s67\n\t"
      "s_addc_u32 s61, s61, s72\n\t"
      "s_add_i32 %[t], %[t], 1\n\t"
      "s_add_i32 s66, %[t], -7\n"
      "2:\n\t"
      "v_cmp_gt_i32 s[64:65], s66, %[vf]\n\t"
      "s_cmp_lg_u64 s[64:65], 0\n\t"
      "s_cbranch_scc1 3f\n"
      "4:\n\t"
      "s_cmp_lg_u32 %[t], %[tend]\n\t"
      "s_cbranch_scc1 1b\n\t"
      "s_branch 8f\n"
      "3:\n\t"
      "s_cmp_eq_u32 s73, 0\n\t"
      "s_cbranch_scc1 4b\n\t"
      "s_sub_u32 s73, s73, 1\n\t"
      "ds_read_b32 %[vf], %[af]\n\t"
      "s_waitcnt lgkmcnt(0)\n\t"
      "s_branch 2b\n"
      "9:\n\t"
      ".set i_, " STR(WMIN) "\n\t.rept " STR(WMAX) "-" STR(WMIN) "+1\n\ts_waitcnt vmcnt(i_)\n\ts_setpc_b64 s[68:69]\n\t.set i_, i_+1\n\t.endr\n"
      "8:\n\t"
      "s_mov_b64 %[p], s[60:61]"
      : [t] "+s"(t), [w] "+s"(w), [pub] "+s"(pub), [sch] "+s"(sch), [p] "+s"(p), [vf] "=&v"(vf), [vt] "=&v"(vt)
      : [slope] "s"(slope), [tend] "s"(tend), [t0] "s"(t0), [af] "v"(a_free), [al] "v"(a_full),
        [v0] "v"(v[0]), [v1] "v"(v[1]), [v2] "v"(v[2]), [v3] "v"(v[3]), [v4] "v"(v[4]), [v5] "v"(v[5]), [v6] "v"(v[6]), [v7] "v"(v[7]),
        [v8] "v"(v[8]), [v9] "v"(v[9]), [v10] "v"(v[10]), [v11] "v"(v[11]), [v12] "v"(v[12]), [v13] "v"(v[13]), [v14] "v"(v[14]), [v15] "v"(v[15])
      : "memory", "scc", "s60", "s61", "s62", "s63", "s64", "s65", "s66", "s67", "s68", "s69", "s70", "s71", "s72", "s73");
}
static_assert(WMAX <= 44 && WMIN >= 13 && WMIN <= W0 && W0 <= WMAX && WMIN <= WSTART && WSTART <= WMAX, "window");
template <int SLEEP> __device__ __forceinline__ unsigned lds_wait(LAS unsigned char* L, int off, unsigned want) {
  unsigned v = 0;
#pragma unroll 1
  for (unsigned s = 0; s < SPIN_CAP; ++s) {
    v = __builtin_amdgcn_readfirstlane(*(volatile LAS unsigned*)(L + off));
    if (v >= want) break;
    if (SLEEP) __builtin_amdgcn_s_sleep(SLEEP);
  }
  asm volatile("" ::: "memory");
  return v;
}
__device__ __forceinline__ int ccol(int n) { return 8 * (n & 7) + 4 * (n >> 3); }
template <int LD> __device__ __forceinline__ int tile_voff(int lane) { return ((32 * ((lane >> 3) & 1) + 8 * (lane >> 4)) * LD + 8 * (lane & 7)) * 2; }
template <int LD> __device__ __forceinline__ void ldtile(u32x4 (&raw)[8], __amdgpu_buffer_rsrc_t r, int elem0, int voff) {
#pragma unroll
  for (int j = 0; j < 8; ++j) raw[j] = __builtin_amdgcn_raw_buffer_load_b128(r, voff, (elem0 + j * LD) * 2, AUX_NT);
}
__device__ __forceinline__ void mkfrag(const u32x4 (&raw)[8], u32x4 (&f0)[4], u32x4 (&f1)[4], int lane) {
  const bool up = lane & 8;
#pragma unroll
  for (int jp = 0; jp < 4; ++jp) {
    unsigned p[8];
#pragma unroll
    for (int d = 0; d < 4; ++d) {
      p[2 * d] = __builtin_amdgcn_perm(raw[2 * jp + 1][d], raw[2 * jp][d], 0x05040100u);
      p[2 * d + 1] = __builtin_amdgcn_perm(raw[2 * jp + 1][d], raw[2 * jp][d], 0x07060302u);
    }
#pragma unroll
    for (int c = 0; c < 4; ++c) {
      const unsigned got = dppu<ROR8>(up ? p[c] : p[4 + c]);
      f0[c][jp] = up ? got : p[c];
      f1[c][jp] = up ? p[4 + c] : got;
    }
  }
  asm volatile("" : "+v"(f0[0]), "+v"(f0[1]), "+v"(f0[2]), "+v"(f0[3]), "+v"(f1[0]), "+v"(f1[1]), "+v"(f1[2]), "+v"(f1[3]));
}
__device__ __forceinline__ void mld4(u32x4& v, u64 m, unsigned voff, const unsigned char* sb) {
  asm volatile("s_mov_b64 exec, %1\n\tglobal_load_dwordx4 %0, %2, %3 sc1\n\ts_mov_b64 exec, -1" : "+v"(v) : "s"(m), "v"(voff), "s"(sb) : "memory");
}
__device__ __forceinline__ void mld2(u32x2& v, u64 m, unsigned voff, const unsigned char* sb) {
  asm volatile("s_mov_b64 exec, %1\n\tglobal_load_dwordx2 %0, %2, %3 sc1\n\ts_mov_b64 exec, -1" : "+v"(v) : "s"(m), "v"(voff), "s"(sb) : "memory");
}
__device__ __forceinline__ u64 inval4(u32x4 v) { return __ballot(max(max(v[0], v[1]), max(v[2], v[3])) == SENT); }
__device__ __forceinline__ u64 inval2(u32x2 v) { return __ballot(max(v[0], v[1]) == SENT); }

#define ST(i) do {} while (0)
#define STY(i) ST(i)

__global__ void __launch_bounds__(NTHR, 3)
fused_block(const bf16_t* __restrict__ x, const bf16_t* __restrict__ kc, const bf16_t* __restrict__ vc,
            const bf16_t* __restrict__ wq, const bf16_t* __restrict__ wo, const bf16_t* __restrict__ wg,
            const bf16_t* __restrict__ wu, const bf16_t* __restrict__ wd, bf16_t* __restrict__ out, unsigned char* ws) {
  extern __shared__ __attribute__((aligned(16))) unsigned char smem[];
  LAS unsigned char* L = (LAS unsigned char*)smem;
  const int tid = threadIdx.x, lane = tid & 63, wave = __builtin_amdgcn_readfirstlane(tid >> 6);
  const int g = blockIdx.x;
  const int bh = g >> 3;
  const __amdgpu_buffer_rsrc_t rs = __builtin_amdgcn_make_buffer_rsrc(ws, 0, W_END, 0x00020000);

  const unsigned t0 = (unsigned)__builtin_amdgcn_s_memrealtime();
  if (wave == 1) {
    ((LAS unsigned*)(L + L_FULL))[lane] = 0u;
    if (lane < 32) ((LAS unsigned*)(L + L_CTL))[lane] = 0u;
    waitl();
  }
  __builtin_amdgcn_s_barrier();
  asm volatile("" ::: "memory");
  if (wave == 0) {
    const size_t chunk = ((size_t)bh * SK + ((g + bh) & 7) * 2048) * (DH * 2);
    unsigned vo[16];
#pragma unroll
    for (int i = 0; i < 16; ++i) vo[i] = lane * 16 + (i & 7) * 8192 + (i >> 3) * 1024 - (i & 3) * 1024;
    const char* p = (const char*)kc + chunk;
    if (LPRIO) __builtin_amdgcn_s_setprio(LPRIO);
    const int tsched = TSCHED - (((g & 31) < 7) ? ADVB : ((g & 63) >= 15 && (g & 63) < 22) ? ADVC : 0);
    int t = 0, pub = 0, w = WSTART, sch = 0, slope = tsched + LEAD * 128 / NRAMP, tend = NRAMP;
#pragma unroll 1
    for (;;) {
      stream_steps(vo, p, t, tend, w, pub, sch, slope, t0, L_FREE + (lane & 3) * 4, L_FULL + lane * 4);
      if (tend == NSTEP) break;
      slope = tsched;
      if (tend == NPH) {
        p = (const char*)vc + chunk;
#pragma unroll
        for (int i = 0; i < 16; ++i) vo[i] ^= (lane >> 5) << 7;
        tend = NSTEP;
      } else {
        tend = NPH;
      }
    }
    waitv<32>(); *(volatile LAS int*)(L + L_FULL) = NSTEP - 2;
    waitv<16>(); *(volatile LAS int*)(L + L_FULL) = NSTEP - 1;
    waitv<0>();  *(volatile LAS int*)(L + L_FULL) = NSTEP;
    ST(3);
  } else if (wave <= 4) {
    const int ci = wave - 1;
    unsigned full = 0;
    volatile LAS unsigned* vfree = (volatile LAS unsigned*)(L + L_FREE) + ci;
    auto fetch = [&](int t, int lo, int o1, int o2, int o3, u32x4 (&d)[4]) {
      if ((int)full <= t) full = lds_wait<CSLEEP>(L, L_FULL, t + 1);
      LAS unsigned char* sb = L + (t & (NSLOT - 1)) * SLOT_BYTES + lo;
      d[0] = *(LAS u32x4*)(sb); d[1] = *(LAS u32x4*)(sb + o1); d[2] = *(LAS u32x4*)(sb + o2); d[3] = *(LAS u32x4*)(sb + o3);
      waitl();
      *vfree = t + 1;
      asm volatile("" : "+v"(d[0]), "+v"(d[1]), "+v"(d[2]), "+v"(d[3])::"memory");
    };
    float M;
    {
      const int r = lane >> 4, c = lane & 15;
      const int lo = (8 * ci + r) * 512 + 16 * c;
      u32x4 pre[PRE][4];
#pragma unroll
      for (int t = 0; t < PRE; ++t) fetch(t, lo, 256, 2048, 2304, pre[t]);
      lds_wait<1>(L, L_QRDY, 1u);
      const u32x4 q0 = *(LAS u32x4*)(L + L_Q + 16 * c), q1 = *(LAS u32x4*)(L + L_Q + 256 + 16 * c);
      constexpr float C2 = 0.0625f * 1.4426950408889634f;
      float m = -INFINITY;
      LAS float* sp = (LAS float*)(L + L_P) + (r + 4 * (c & 1)) * 4 + ci;
      auto score = [&](int t, const u32x4 (&k)[4]) {
        float s0 = 0.f, s1 = 0.f;
#pragma unroll
        for (int j = 0; j < 4; ++j) { s0 = dot2(k[0][j], q0[j], s0); s1 = dot2(k[2][j], q0[j], s1); }
#pragma unroll
        for (int j = 0; j < 4; ++j) { s0 = dot2(k[1][j], q1[j], s0); s1 = dot2(k[3][j], q1[j], s1); }
        s0 += dpp<XOR1>(s0); s1 += dpp<XOR1>(s1);
        float s = (c & 1) ? s1 : s0;
        s += dpp<XOR2>(s); s += dpp<ROR4>(s); s += dpp<ROR8>(s);
        s = rbf(s) * C2;
        m = fmaxf(m, s);
        if (c < 2) sp[t * 32] = s;
      };
#pragma unroll
      for (int t = 0; t < PRE; ++t) score(t, pre[t]);
#pragma unroll 1
      for (int t = PRE; t < NPH; ++t) { u32x4 k[4]; fetch(t, lo, 256, 2048, 2304, k); score(t, k); }
      m = fmaxf(m, dpp<XOR1>(m));
      m = x32_max(x16_max(m));
      if (lane == 0) ((LAS float*)(L + L_MW))[ci] = m;
      waitl();
      if (lane == 0) __hip_atomic_fetch_add((LAS unsigned*)(L + L_C1), 1u, RLX_WG);
      lds_wait<0>(L, L_C1, 4u);
      const f32x4 mw = *(LAS f32x4*)(L + L_MW);
      M = fmaxf(fmaxf(mw[0], mw[1]), fmaxf(mw[2], mw[3]));
      LAS f32x4* pp = (LAS f32x4*)(L + L_P) + ci * 128 + lane;
      f32x4 e0 = pp[0], e1 = pp[64];
      float l = 0.f;
#pragma unroll
      for (int j = 0; j < 4; ++j) { e0[j] = __builtin_amdgcn_exp2f(e0[j] - M); e1[j] = __builtin_amdgcn_exp2f(e1[j] - M); l += e0[j] + e1[j]; }
      pp[0] = e0; pp[64] = e1;
      l = wave_sum(l);
      if (lane == 0) ((LAS float*)(L + L_LW))[ci] = l;
      waitl();
      if (lane == 0) __hip_atomic_fetch_add((LAS unsigned*)(L + L_C2), 1u, RLX_WG);
      lds_wait<0>(L, L_C2, 4u);
    }
    {
      const int kr = lane >> 3, c = lane & 7;
      const int lo = kr * 512 + (ci ^ (kr & 1)) * 128 + 16 * c;
      float acc[8];
#pragma unroll
      for (int j = 0; j < 8; ++j) acc[j] = 0.f;
      int pass = DRYRUN ? 0 : 1;
#pragma unroll 1
      for (; pass < 2; ++pass) {
        asm volatile("" : "+s"(pass));
        float sc = 0.f;
        if (pass) {
#pragma unroll 1
          for (int t = NPH; t < NSTEP; ++t) {
            const f32x4 pv = *(LAS f32x4*)(L + L_P + ((t - NPH) * 32 + kr * 4) * 4);
            u32x4 v[4];
            fetch(t, lo, 4096, 8192, 12288, v);
#pragma unroll
            for (int j = 0; j < 4; ++j)
#pragma unroll
              for (int i = 0; i < 4; ++i) {
                acc[2 * i] = fmaf(pv[j], bflo(v[j][i]), acc[2 * i]);
                acc[2 * i + 1] = fmaf(pv[j], bfhi(v[j][i]), acc[2 * i + 1]);
              }
            if (t == NSTEP - 4) { lds_wait<1>(L, L_SCRDY, 1u); sc = *(LAS float*)(L + L_SCALE); }
          }
          if (wave == 1) ST(4);
        }
        float a4[4], a2[2];
#pragma unroll
        for (int j = 0; j < 4; ++j) a4[j] = tr32(acc[j], acc[j + 4]);
#pragma unroll
        for (int j = 0; j < 2; ++j) { a2[j] = tr16(a4[j], a4[j + 2]); a2[j] += dpp<ROR8>(a2[j]); }
        const int off = W_RECA + g * RECA + (64 * ci + 8 * c + 4 * (lane >> 5) + 2 * ((lane >> 4) & 1)) * 2;
        __builtin_amdgcn_raw_buffer_store_b32(canonu(pk2(a2[0] * sc, a2[1] * sc)), rs, (lane & 8) || !pass ? OOB : off, 0, AUX_SC1);
      }
      if (wave == 1) ST(5);
    }
  } else {
    const int tw = wave - 5;
    const bool role0 = (g & 31) < 7 || ((g & 63) >= 15 && (g & 63) < 22);
    u64 tag;
    {
      const int hq = 2 * (g & 7) + (g >> 7), bq = (g >> 3) & 1, cq = (g >> 4) & 7;
      u32x4 wqr[4], xall;
      unsigned xr[4];
      const int row0 = 64 * tw + (lane >> 2);
      xall = *(const GAS u32x4*)(x + bq * D + 8 * min(lane, 55));
#pragma unroll
      for (int i = 0; i < 4; ++i) xr[i] = *(const GAS unsigned short*)(x + bq * D + row0 + 16 * i);
#pragma unroll
      for (int i = 0; i < 4; ++i)
        wqr[i] = __builtin_nontemporal_load((const GAS u32x4*)(wq + (size_t)(row0 + 16 * i) * HD + hq * DH + 32 * cq + 8 * (lane & 3)));
      unsigned z = 0;
      asm volatile("" : "+v"(z));
      const u64 gen = __hip_atomic_load((GAS u64*)(ws + W_GEN + z), RLX_AGENT);
      __builtin_amdgcn_sched_barrier(0);
      if (lane >= 56) xall = u32x4{0u, 0u, 0u, 0u};
      float xs[8]; float s = 0.f;
#pragma unroll
      for (int i = 0; i < 4; ++i) { xs[2 * i] = bflo(xall[i]); xs[2 * i + 1] = bfhi(xall[i]); s += xs[2 * i] + xs[2 * i + 1]; }
      const float mean = wave_sum(s) * (1.f / D);
      float qv = 0.f;
#pragma unroll
      for (int i = 0; i < 8; ++i) { const float d = xs[i] - mean; qv = fmaf(d, d, qv); }
      if (lane >= 56) qv = 0.f;
      const float var = wave_sum(qv) * (1.f / D);
      const float mb = rbf(mean);
      const float rsd = __builtin_amdgcn_rcpf(rbf(__builtin_amdgcn_sqrtf(rbf(rbf(var) + 1e-5f))));
      float a[8];
#pragma unroll
      for (int j = 0; j < 8; ++j) a[j] = 0.f;
#pragma unroll
      for (int i = 0; i < 4; ++i) {
        const float xn = rbf(rbf(bflo(xr[i]) - mb) * rsd);
#pragma unroll
        for (int j = 0; j < 4; ++j) { a[2 * j] = fmaf(xn, bflo(wqr[i][j]), a[2 * j]); a[2 * j + 1] = fmaf(xn, bfhi(wqr[i][j]), a[2 * j + 1]); }
      }
      float b4[4], b2[2];
#pragma unroll
      for (int j = 0; j < 4; ++j) b4[j] = tr32(a[j], a[j + 4]);
#pragma unroll
      for (int j = 0; j < 2; ++j) { b2[j] = tr16(b4[j], b4[j + 2]); b2[j] += dpp<ROR4>(b2[j]); b2[j] += dpp<ROR8>(b2[j]); }
      if ((lane & 12) == 0) {
        const int col = 8 * (lane & 3) + 4 * (lane >> 5) + 2 * ((lane >> 4) & 1);
        *(LAS f32x2*)(L + L_QPART + (tw * 32 + col) * 4) = f32x2{b2[0], b2[1]};
      }
      waitl();
      if (lane == 0) __hip_atomic_fetch_add((LAS unsigned*)(L + L_QCNT), 1u, RLX_WG);
      tag = mix64(((u64)__builtin_amdgcn_readfirstlane((unsigned)(gen >> 32)) << 32) | __builtin_amdgcn_readfirstlane((unsigned)gen));
      if (tw == 0) {
        lds_wait<0>(L, L_QCNT, 7u);
        ST(1);
        const u64 t48 = tag & ~0xFFFFull;
        if (lane < 32) {
          float qs = 0.f;
#pragma unroll
          for (int w = 0; w < 7; ++w) qs += ((LAS float*)(L + L_QPART))[w * 32 + lane];
          __hip_atomic_store((GAS u64*)(ws + W_QG) + bq * HD + hq * DH + 32 * cq + lane, t48 | f2bf(qs), RLX_AGENT);
        }
        GAS u64* qg = (GAS u64*)(ws + W_QG) + bh * DH + lane;
        u64 v[4];
#pragma unroll 1
        for (unsigned spins = 0; spins < SPIN_CAP; ++spins) {
          bool ok = true;
#pragma unroll
          for (int i = 0; i < 4; ++i) v[i] = __hip_atomic_load(qg + 64 * i, RLX_AGENT);
#pragma unroll
          for (int i = 0; i < 4; ++i) ok &= (v[i] & ~0xFFFFull) == t48;
          if (__all(ok)) break;
        }
#pragma unroll
        for (int i = 0; i < 4; ++i) ((LAS unsigned short*)(L + L_Q))[64 * i + lane] = (unsigned short)v[i];
        waitl();
        *(volatile LAS unsigned*)(L + L_QRDY) = 1u;
        ST(2);
      }
    }
    int pass = __builtin_amdgcn_readfirstlane((DRYRUN && role0) ? 0 : 1);
    if (pass == 0) lds_wait<8>(L, L_FULL, 20u);
    const int lane_ = lane, g_ = g, tw_ = tw;
    unsigned char* const ws_ = ws;
#pragma unroll 1
    for (; pass < 2; ++pass) {
      int lane = lane_, g = g_, tw = tw_;
      unsigned char* ws = ws_;
      asm volatile("" : "+s"(pass), "+v"(lane), "+s"(g), "+s"(tw), "+s"(ws));
      const int n = lane & 15, kg = lane >> 4, bh = g >> 3;
      const bool roleB = (g & 31) < 7;
      const bool roleC = (g & 63) >= 15 && (g & 63) < 22;
      const int ks = g >> 5, cs = g & 31, jc = (g >> 6) * 7 + (g & 63) - 15;
      if (pass && tw == 1) {
        lds_wait<8>(L, L_C2, 4u);
        const u32x4 sv = {SENT, SENT, SENT, SENT};
        __builtin_amdgcn_raw_buffer_store_b128(sv, rs, lane < 32 ? W_RECA + g * RECA + 16 * lane : OOB, 0, AUX_SC1);
        if (roleB) __builtin_amdgcn_raw_buffer_store_b128(sv, rs, lane < 32 ? W_RECB + ((ks * 2 + kg) * D + 64 * cs + 4 * n) * 4 : OOB, 0, AUX_SC1);
        if (roleC) {
#pragma unroll
          for (int i = 0; i < 4; ++i) __builtin_amdgcn_raw_buffer_store_b128(sv, rs, lane < 56 ? W_RECC + jc * (2 * D * 4) + (56 * i + lane) * 16 : OOB, 0, AUX_SC1);
        }
        const f32x4 mw = *(LAS f32x4*)(L + L_MW), lw = *(LAS f32x4*)(L + L_LW);
        const float Mw = fmaxf(fmaxf(mw[0], mw[1]), fmaxf(mw[2], mw[3])), Lw = (lw[0] + lw[1]) + (lw[2] + lw[3]);
        __builtin_amdgcn_raw_buffer_store_b64(u32x2{__float_as_uint(Mw), __float_as_uint(Lw)}, rs, lane == 0 ? W_ML + g * 8 : OOB, 0, AUX_SC1);
        waitv<0>();
        if (lane == 0) __hip_atomic_store((GAS u64*)(ws + W_FLAG) + g, tag, RLX_AGENT);
        lds_wait<16>(L, L_FULL, ML_STEP);
        const bool all = roleB || roleC;
        GAS u64* fp = (GAS u64*)(ws + W_FLAG) + (all ? lane : 8 * bh + (lane & 7));
        const int fs = all ? 64 : 0;
#pragma unroll 1
        for (unsigned spins = 0; spins < SPIN_CAP; ++spins) {
          bool ok = true;
#pragma unroll
          for (int i = 0; i < 4; ++i) ok &= __hip_atomic_load(fp + fs * i, RLX_AGENT) == tag;
          if (__all(ok)) break;
          __builtin_amdgcn_s_sleep(16);
        }
        const u64 ml = __hip_atomic_load((GAS u64*)(ws + W_ML) + 8 * bh + (lane & 7), RLX_AGENT);
        const float mi = __uint_as_float((unsigned)ml), li = __uint_as_float((unsigned)(ml >> 32));
        float Mg = fmaxf(mi, dpp<XOR1>(mi)); Mg = fmaxf(Mg, dpp<XOR2>(Mg)); Mg = fmaxf(Mg, dpp<HMIR>(Mg));
        float Lg = li * __builtin_amdgcn_exp2f(mi - Mg);
        Lg += dpp<XOR1>(Lg); Lg += dpp<XOR2>(Lg); Lg += dpp<HMIR>(Lg);
        *(volatile LAS float*)(L + L_SCALE) = __builtin_amdgcn_exp2f(Mw - Mg) / Lg;
        waitl();
        *(volatile LAS unsigned*)(L + L_SCRDY) = 1u;
        if (g == 0 && lane == 0) __hip_atomic_store((GAS u64*)(ws + W_GEN), tag, RLX_AGENT);
      }
      const unsigned cap = pass ? SPIN_CAP : 1u;
      const int dmask = pass ? 0 : OOB;
      if (roleB) {
        if (tw < 4) {
          if (pass) lds_wait<16>(L, L_FULL, TWB + TWS * tw);
          u32x4 fr[4][4];
          {
            const __amdgpu_buffer_rsrc_t r = __builtin_amdgcn_make_buffer_rsrc((void*)wo, 0, HD * D * 2, 0x00020000);
            u32x4 raw[2][8];
            const int voff = tile_voff<D>(lane) | dmask;
#pragma unroll
            for (int s = 0; s < 2; ++s) ldtile<D>(raw[s], r, (512 * ks + 128 * tw + 64 * s) * D + 64 * cs, voff);
#pragma unroll
            for (int s = 0; s < 2; ++s) mkfrag(raw[s], fr[2 * s], fr[2 * s + 1], lane);
          }
          const unsigned off = W_RECA + (((n & 1) * 16 + 2 * ks + (tw >> 1)) * 8 + (n >> 1)) * RECA + (128 * (tw & 1) + 8 * kg) * 2;
          u32x4 v[4];
#pragma unroll
          for (int i = 0; i < 4; ++i) v[i] = u32x4{SENT, SENT, SENT, SENT};
          if (pass) { lds_wait<4>(L, L_SCRDY, 1u); lds_wait<1>(L, L_FULL, NSTEP); }
#pragma unroll 1
          for (unsigned spins = 0; spins < cap; ++spins) {
            u64 mk[4], any = 0;
#pragma unroll
            for (int i = 0; i < 4; ++i) { mk[i] = inval4(v[i]); any |= mk[i]; }
            if (any == 0) break;
#pragma unroll
            for (int i = 0; i < 4; ++i) mld4(v[i], mk[i], off, ws + i * 64);
            asm volatile("s_waitcnt vmcnt(0)" : "+v"(v[0]), "+v"(v[1]), "+v"(v[2]), "+v"(v[3])::"memory");
          }
          if (tw == 0 && pass) STY(6);
          f32x4 d[4];
#pragma unroll
          for (int c = 0; c < 4; ++c) d[c] = mfma(v[0], fr[0][c], f32x4{0.f, 0.f, 0.f, 0.f});
#pragma unroll
          for (int s = 1; s < 4; ++s)
#pragma unroll
            for (int c = 0; c < 4; ++c) d[c] = mfma(v[s], fr[s][c], d[c]);
          float e[4];
#pragma unroll
          for (int c = 0; c < 4; ++c) e[c] = tr32(d[c][0] + d[c][2], d[c][1] + d[c][3]);
          const f32x2 o = {tr16(e[0], e[2]), tr16(e[1], e[3])};
          *(LAS f32x2*)(L + L_RED + ((tw * 2 + (lane >> 5)) * 64 + ccol(n) + 2 * (kg & 1)) * 4) = o;
        }
        if (pass) bar();
        if (tw == 0) {
          f32x2 s = {0.f, 0.f};
#pragma unroll
          for (int w2 = 0; w2 < 4; ++w2) s += *(LAS f32x2*)(L + L_RED + (w2 * 128 + 2 * lane) * 4);
          __builtin_amdgcn_raw_buffer_store_b64(u32x2{canon(s[0]), canon(s[1])}, rs,
                                                (W_RECB + ((ks * 2 + (lane >> 5)) * D + 64 * cs + 2 * (lane & 31)) * 4) | dmask, 0, AUX_SC1);
          if (pass) STY(7);
        }
      } else if (roleC) {
        if (pass) lds_wait<16>(L, L_FULL, TWC + TWS * tw);
        u32x4 fr[6][4];
        {
          const __amdgpu_buffer_rsrc_t r1 = __builtin_amdgcn_make_buffer_rsrc((void*)wg, 0, FF * D * 2, 0x00020000);
          const __amdgpu_buffer_rsrc_t r2 = __builtin_amdgcn_make_buffer_rsrc((void*)wu, 0, FF * D * 2, 0x00020000);
          const __amdgpu_buffer_rsrc_t r3 = __builtin_amdgcn_make_buffer_rsrc((void*)wd, 0, FF * D * 2, 0x00020000);
          u32x4 raw[3][8];
          const int vf = tile_voff<FF>(lane) | dmask, vd = tile_voff<D>(lane) | dmask;
          ldtile<FF>(raw[0], r1, 64 * tw * FF + 64 * jc, vf);
          ldtile<FF>(raw[1], r2, 64 * tw * FF + 64 * jc, vf);
          ldtile<D>(raw[2], r3, 64 * jc * D + 64 * tw, vd);
#pragma unroll
          for (int i = 0; i < 3; ++i) mkfrag(raw[i], fr[2 * i], fr[2 * i + 1], lane);
        }
        const int b = n & 1, sub = n >> 1;
        const int col = 64 * tw + 32 * (sub >> 2) + 8 * kg + 2 * (sub & 3);
        unsigned xp = *(const GAS unsigned*)(x + b * D + col);
        asm volatile("" : "+v"(xp));
        const unsigned off = W_RECB + (b * D + col) * 4;
        u32x2 v[8];
#pragma unroll
        for (int i = 0; i < 8; ++i) v[i] = u32x2{SENT, SENT};
        if (pass) { lds_wait<4>(L, L_SCRDY, 1u); lds_wait<1>(L, L_FULL, NSTEP); }
#pragma unroll 1
        for (unsigned spins = 0; spins < cap; ++spins) {
          u64 mk[8], any = 0;
#pragma unroll
          for (int i = 0; i < 8; ++i) { mk[i] = inval2(v[i]); any |= mk[i]; }
          if (any == 0) break;
#pragma unroll
          for (int i = 0; i < 8; ++i) mld2(v[i], mk[i], off, ws + i * (2 * D * 4));
          asm volatile("s_waitcnt vmcnt(0)" : "+v"(v[0]), "+v"(v[1]), "+v"(v[2]), "+v"(v[3]), "+v"(v[4]), "+v"(v[5]), "+v"(v[6]), "+v"(v[7])::"memory");
        }
        if (tw == 0 && pass) STY(8);
        {
          f32x2 a = {0.f, 0.f};
#pragma unroll
          for (int i = 0; i < 8; i += 2) a += __builtin_bit_cast(f32x2, v[i]) + __builtin_bit_cast(f32x2, v[i + 1]);
          const unsigned ab = pk2(a[0], a[1]);
          const unsigned hp = pk2(bflo(xp) + bflo(ab), bfhi(xp) + bfhi(ab));
          u32x4 a0 = {hp, dppu<SHL + 2>(hp), dppu<SHL + 4>(hp), dppu<SHL + 6>(hp)};
          u32x4 a1 = {dppu<SHL + 8>(hp), dppu<SHL + 10>(hp), dppu<SHL + 12>(hp), dppu<SHL + 14>(hp)};
          if (n == 2) { a0 = u32x4{0x3F803F80u, 0x3F803F80u, 0x3F803F80u, 0x3F803F80u}; a1 = a0; }
          f32x4 dg[4], du[4];
#pragma unroll
          for (int c = 0; c < 4; ++c) { dg[c] = mfma(a0, fr[0][c], f32x4{0.f, 0.f, 0.f, 0.f}); du[c] = mfma(a0, fr[2][c], f32x4{0.f, 0.f, 0.f, 0.f}); }
#pragma unroll
          for (int c = 0; c < 4; ++c) { dg[c] = mfma(a1, fr[1][c], dg[c]); du[c] = mfma(a1, fr[3][c], du[c]); }
          const unsigned p0 = __builtin_amdgcn_readlane(hp, 0), p1 = __builtin_amdgcn_readlane(hp, 1);
          const float pv = bflo(b ? p1 : p0);
          const float d0 = bflo(hp) - pv, d1 = bfhi(hp) - pv;
          float s1 = d0 + d1, s2 = fmaf(d0, d0, d1 * d1);
          s1 = tr32(s1, s2);
          s1 = x16_sum(s1);
          s1 += dpp<XOR2>(s1); s1 += dpp<ROR4>(s1); s1 += dpp<ROR8>(s1);
          *(LAS unsigned*)(L + L_H + (b * D + col) * 2) = hp;
          if ((lane & 14) == 0) ((LAS float*)(L + L_ST))[(b * 8 + tw) * 4 + kg] = kg == 1 ? pv : s1;
          if (kg == 0) {
#pragma unroll
            for (int r = 0; r < 3; ++r) {
              *(LAS f32x4*)(L + L_RED + ((tw * 6 + r) * 64 + ccol(n)) * 4) = f32x4{dg[0][r], dg[1][r], dg[2][r], dg[3][r]};
              *(LAS f32x4*)(L + L_RED + ((tw * 6 + 3 + r) * 64 + ccol(n)) * 4) = f32x4{du[0][r], du[1][r], du[2][r], du[3][r]};
            }
          }
        }
        if (pass) bar();
        if (tw == 0 && pass) ST(12);
        if (tw < 2) {
          float sg = 0.f, su = 0.f, cg = 0.f, cu = 0.f;
          const LAS float* rp = (const LAS float*)(L + L_RED) + lane;
#pragma unroll
          for (int w2 = 0; w2 < 7; ++w2) {
            sg += rp[(w2 * 6) * 64 + tw * 64]; cg += rp[(w2 * 6 + 2) * 64];
            su += rp[(w2 * 6 + 3) * 64 + tw * 64]; cu += rp[(w2 * 6 + 5) * 64];
          }
          const f32x4 st = *(const LAS f32x4*)(L + L_ST + (tw * 8 + min(lane & 7, 6)) * 16);
          const bool live = (lane & 7) < 7;
          const float mw = st[1] + st[0] * (1.f / 64.f);
          float ms = live ? mw : 0.f;
          ms += dpp<XOR1>(ms); ms += dpp<XOR2>(ms); ms += dpp<HMIR>(ms);
          const float mean = ms * (1.f / 7.f);
          const float dm = mw - mean;
          float m2 = live ? fmaf(64.f * dm, dm, st[2] - st[0] * st[0] * (1.f / 64.f)) : 0.f;
          m2 += dpp<XOR1>(m2); m2 += dpp<XOR2>(m2); m2 += dpp<HMIR>(m2);
          const float mb = rbf(mean);
          const float rsd = __builtin_amdgcn_rcpf(rbf(__builtin_amdgcn_sqrtf(rbf(rbf(m2 * (1.f / D)) + 1e-5f))));
          const float xg = rbf((sg - mb * cg) * rsd), xu = rbf((su - mb * cu) * rsd);
          const float u2 = (2.f * 0.7978845608028654f * 1.4426950408889634f) * (xg + 0.044715f * xg * xg * xg);
          const float ge = xg * __builtin_amdgcn_rcpf(1.f + __builtin_amdgcn_exp2f(-u2));
          *(LAS unsigned short*)(L + L_G + (tw * 64 + lane) * 2) = (unsigned short)f2bf(rbf(ge) * xu);
        }
        if (pass) bar();
        if (tw == 0 && pass) ST(13);
        {
          const LAS unsigned char* ap = L + L_G + (b * 64 + 8 * kg) * 2;
          const u32x4 a0 = *(const LAS u32x4*)ap, a1 = *(const LAS u32x4*)(ap + 64);
          f32x4 d[4];
#pragma unroll
          for (int c = 0; c < 4; ++c) d[c] = mfma(a0, fr[4][c], f32x4{0.f, 0.f, 0.f, 0.f});
#pragma unroll
          for (int c = 0; c < 4; ++c) d[c] = mfma(a1, fr[5][c], d[c]);
          u32x4 o;
#pragma unroll
          for (int c = 0; c < 4; ++c) o[c] = canon((lane & 16) ? d[c][1] : d[c][0]);
          __builtin_amdgcn_raw_buffer_store_b128(o, rs, lane < 32 ? (W_RECC + ((jc * 2 + kg) * D + 64 * tw + ccol(n)) * 4) | dmask : OOB, 0, AUX_SC1);
          if (tw == 0 && pass) ST(9);
        }
        if (tw == 6) {
          const int pg = lane >> 3, ch = lane & 7;
          const unsigned offd = W_RECC + pg * (2 * D * 4) + (32 * jc + 4 * ch) * 4;
          u32x4 w[4];
#pragma unroll
          for (int i = 0; i < 4; ++i) { const unsigned iv = (i < 3 || pg < 4) ? SENT : 0u; w[i] = u32x4{iv, iv, iv, iv}; }
          const u32x2 hh = *(LAS u32x2*)(L + L_H + (32 * jc + 4 * ch) * 2);
#pragma unroll 1
          for (unsigned spins = 0; spins < cap; ++spins) {
            u64 mk[4], any = 0;
#pragma unroll
            for (int i = 0; i < 4; ++i) { mk[i] = inval4(w[i]); any |= mk[i]; }
            if (any == 0) break;
#pragma unroll
            for (int i = 0; i < 4; ++i) mld4(w[i], mk[i], offd, ws + i * 8 * (2 * D * 4));
            asm volatile("s_waitcnt vmcnt(0)" : "+v"(w[0]), "+v"(w[1]), "+v"(w[2]), "+v"(w[3])::"memory");
          }
          if (pass) ST(10);
          const f32x4 t = (__builtin_bit_cast(f32x4, w[0]) + __builtin_bit_cast(f32x4, w[1])) + (__builtin_bit_cast(f32x4, w[2]) + __builtin_bit_cast(f32x4, w[3]));
          float s0 = tr32(t[0], t[2]), s1 = tr32(t[1], t[3]);
          s0 = x16_sum(s0); s1 = x16_sum(s1);
          s0 += dpp<ROR8>(s0); s1 += dpp<ROR8>(s1);
          const unsigned hv = (lane & 32) ? hh[1] : hh[0];
          const unsigned dn = pk2(s0, s1);
          const __amdgpu_buffer_rsrc_t ro = __builtin_amdgcn_make_buffer_rsrc((void*)out, 0, 2 * D * 2, 0x00020000);
          __builtin_amdgcn_raw_buffer_store_b32(pk2(bflo(hv) + bflo(dn), bfhi(hv) + bfhi(dn)), ro,
                                                (lane & 24) ? OOB : ((32 * jc + 4 * ch + 2 * (lane >> 5)) * 2) | dmask, 0, 0);
          if (pass) ST(11);
        }
      }
    }
  }
}

extern "C" void kernel_launch(void* const* d_in, const int* in_sizes, int n_in, void* d_out, int out_size, void* d_ws,
                              size_t ws_size, hipStream_t stream) {
  (void)hipFuncSetAttribute((const void*)fused_block, hipFuncAttributeMaxDynamicSharedMemorySize, LDS_BYTES);
  hipLaunchKernelGGL(fused_block, dim3(NWG), dim3(NTHR), LDS_BYTES, stream, (const bf16_t*)d_in[0], (const bf16_t*)d_in[1],
                     (const bf16_t*)d_in[2], (const bf16_t*)d_in[3], (const bf16_t*)d_in[4], (const bf16_t*)d_in[6],
                     (const bf16_t*)d_in[7], (const bf16_t*)d_in[8], (bf16_t*)d_out, (unsigned char*)d_ws);
}
```

```cpp
#include <hip/hip_runtime.h>
#include <hip/hip_bf16.h>
#include <stdint.h>
#include <type_traits>

typedef unsigned short bf16_t;
typedef unsigned long long u64;
typedef unsigned u32x4 __attribute__((ext_vector_type(4)));
typedef unsigned u32x2 __attribute__((ext_vector_type(2)));
typedef float f32x4 __attribute__((ext_vector_type(4)));
typedef float f32x2 __attribute__((ext_vector_type(2)));
typedef __bf16 bf16x2 __attribute__((ext_vector_type(2)));
typedef __bf16 bf16x8 __attribute__((ext_vector_type(8)));
#define LAS __attribute__((address_space(3)))
#define GAS __attribute__((address_space(1)))
#define RLX_AGENT __ATOMIC_RELAXED, __HIP_MEMORY_SCOPE_AGENT
#define RLX_WG __ATOMIC_RELAXED, __HIP_MEMORY_SCOPE_WORKGROUP

constexpr int D = 448, SK = 16384, DH = 256, HD = 4096, FF = 1792;
constexpr int NWG = 256, NTHR = 768;
constexpr int NPH = 64, NSTEP = 2 * NPH;
constexpr int NSLOT = 8, SLOT_BYTES = 16384;
#define W0 19
#define WMIN 15
#define WMAX 32
#define TSCHED 6940
#define LEAD 200
#define NCP 4
#define CP0 14
#define CPS 30
#define CPD 6
#define PMODE 1
#define STAG 8
#define PF_STEP 0
#define CWDELAY 0
#define TWSLEEP 2
#define RB 1
#define SB 0
#define RC 1
#define SC 0
#define RD 1
#define SD 0
#define WDLATE 1
#define JUNK 1
#define CWAIT 0
#define WARMC 120
#define BIAS 40
#define WSTART 28
#define ADVB 0
#define ADVC 0
#define GAIN 8
#define PRE 8
#define TWB 24
#define TWC 36
#define TWS 0
#define ML_STEP 96
#define LPRIO 3
#define CSLEEP 1
#define VSLEEP 1
#define BDELAY 0
#define CDELAY 0
#define DDELAY 6
#define CXCD 7
constexpr int L_CTL = NSLOT * SLOT_BYTES;
constexpr int L_FREE = L_CTL, L_QCNT = L_CTL + 20, L_QRDY = L_CTL + 24, L_C1 = L_CTL + 28, L_C2 = L_CTL + 32,
              L_SCRDY = L_CTL + 36, L_SCALE = L_CTL + 40, L_PUB = L_CTL + 44, L_CORR = L_CTL + 48, L_MW = L_CTL + 64, L_LW = L_CTL + 80;
constexpr int L_FULL = L_CTL + 128;
constexpr int L_E = L_FULL + 256;
constexpr int L_QPART = L_E + 256;
constexpr int L_Q = L_QPART + 1024;
constexpr int L_P = L_Q + 512;
constexpr int L_ZERO = L_P + 8192;
constexpr int L_RED = L_ZERO + 128;
constexpr int L_ST = L_RED + 10752;
constexpr int L_H = L_ST + 128;
constexpr int L_G = L_H + 1792;
constexpr int L_DBG = L_G + 256;
constexpr int LDS_BYTES = L_DBG + 256;
static_assert(LDS_BYTES <= 160 * 1024, "LDS");
constexpr int W_GEN = 0;
constexpr int W_FLAG = 4096;
constexpr int W_ML = 8192;
constexpr int W_PROG = 12288;
constexpr int W_QG = W_PROG + 16384;
constexpr int W_RECA = W_QG + 65536, RECA = 512;
constexpr int W_RECB = W_RECA + 256 * RECA;
constexpr int W_RECC = W_RECB + 8 * 2 * D * 4;
constexpr int W_END = W_RECC + 28 * 2 * D * 4;
constexpr int W_STAMP = 1 << 22;
constexpr unsigned SENT = 0xFFFFFFFFu;
constexpr int OOB = 0x40000000;
constexpr int AUX_SC1 = 16, AUX_NT = 2;
constexpr unsigned SPIN_CAP = 1u << 20;

__device__ __forceinline__ float bflo(unsigned v) { return __builtin_bit_cast(float, v << 16); }
__device__ __forceinline__ float bfhi(unsigned v) { return __builtin_bit_cast(float, v & 0xffff0000u); }
__device__ __forceinline__ unsigned f2bf(float f) { return __builtin_bit_cast(unsigned short, (__bf16)f); }
__device__ __forceinline__ float rbf(float f) { return bflo(f2bf(f)); }
__device__ __forceinline__ unsigned pk2(float lo, float hi) {
  bf16x2 v = {(__bf16)lo, (__bf16)hi};
  return __builtin_bit_cast(unsigned, v);
}
__device__ __forceinline__ float dot2(unsigned k, unsigned q, float acc) {
  return __builtin_amdgcn_fdot2_f32_bf16(__builtin_bit_cast(bf16x2, k), __builtin_bit_cast(bf16x2, q), acc, false);
}
template <int CTRL> __device__ __forceinline__ unsigned dppu(unsigned x) { return __builtin_amdgcn_mov_dpp((int)x, CTRL, 0xf, 0xf, true); }
template <int CTRL> __device__ __forceinline__ float dpp(float x) { return __uint_as_float(dppu<CTRL>(__float_as_uint(x))); }
constexpr int XOR1 = 0xB1, XOR2 = 0x4E, HMIR = 0x141, MIR = 0x140, ROR4 = 0x124, ROR8 = 0x128, SHL = 0x100;
__device__ __forceinline__ float row16_sum(float x) {
  x += dpp<XOR1>(x); x += dpp<XOR2>(x); x += dpp<HMIR>(x); x += dpp<MIR>(x);
  return x;
}
__device__ __forceinline__ float tr32(float a, float b) {
  auto s = __builtin_amdgcn_permlane32_swap(__float_as_uint(a), __float_as_uint(b), false, false);
  return __uint_as_float(s[0]) + __uint_as_float(s[1]);
}
__device__ __forceinline__ float tr16(float a, float b) {
  auto s = __builtin_amdgcn_permlane16_swap(__float_as_uint(a), __float_as_uint(b), false, false);
  return __uint_as_float(s[0]) + __uint_as_float(s[1]);
}
__device__ __forceinline__ float x16_sum(float x) { return tr16(x, x); }
__device__ __forceinline__ float x32_sum(float x) { return tr32(x, x); }
__device__ __forceinline__ float x16_max(float x) {
  auto s = __builtin_amdgcn_permlane16_swap(__float_as_uint(x), __float_as_uint(x), false, false);
  return fmaxf(__uint_as_float(s[0]), __uint_as_float(s[1]));
}
__device__ __forceinline__ float x32_max(float x) {
  auto s = __builtin_amdgcn_permlane32_swap(__float_as_uint(x), __float_as_uint(x), false, false);
  return fmaxf(__uint_as_float(s[0]), __uint_as_float(s[1]));
}
__device__ __forceinline__ float wave_sum(float x) { return x32_sum(x16_sum(row16_sum(x))); }
__device__ __forceinline__ unsigned canonu(unsigned u) { return min(u, SENT - 1u); }
__device__ __forceinline__ unsigned canon(float f) { return canonu(__float_as_uint(f)); }
__device__ __forceinline__ f32x4 mfma(u32x4 a, u32x4 b, f32x4 c) {
  return __builtin_amdgcn_mfma_f32_16x16x32_bf16(__builtin_bit_cast(bf16x8, a), __builtin_bit_cast(bf16x8, b), c, 0, 0, 0);
}
template <int N> __device__ __forceinline__ void waitv() { asm volatile("s_waitcnt vmcnt(%0)" ::"i"(N) : "memory"); }
__device__ __forceinline__ void waitl() { asm volatile("s_waitcnt lgkmcnt(0)" ::: "memory"); }
__device__ __forceinline__ void bar() { waitl(); __builtin_amdgcn_s_barrier(); asm volatile("" ::: "memory"); }
__device__ __forceinline__ u64 mix64(u64 z) {
  z += 0x9E3779B97F4A7C15ull; z = (z ^ (z >> 30)) * 0xBF58476D1CE4E5B9ull; z = (z ^ (z >> 27)) * 0x94D049BB133111EBull;
  return z ^ (z >> 31);
}
#define STR2(x) #x
#define STR(x) STR2(x)
#define NTS " nt"
#define BIGINC "0xe800"
#define BIGHI ""
#define DMA4(a, b, c, d) \
  "global_load_lds_dwordx4 %[" #a "], s[60:61] offset:0" NTS "\n\t" \
  "global_load_lds_dwordx4 %[" #b "], s[60:61] offset:1024" NTS "\n\t" \
  "global_load_lds_dwordx4 %[" #c "], s[60:61] offset:2048" NTS "\n\t" \
  "global_load_lds_dwordx4 %[" #d "], s[60:61] offset:3072" NTS "\n\t"
__device__ __forceinline__ void stream_steps(const unsigned (&v)[16], const char* p, int& t, int tend, int& w, int& pub, int& sch,
                                             unsigned a_free, unsigned a_full) {
  unsigned vf, vt, ve;
  asm volatile(
      "s_mov_b64 s[60:61], %[p]\n\t"
      "s_getpc_b64 s[62:63]\n"
      "0:\n\t"
      "s_add_u32 s62, s62, 9f-0b-8*" STR(WMIN) "\n\t"
      "s_addc_u32 s63, s63, 0\n\t"
      "s_mov_b32 s73, 0x100000\n"
      "1:\n\t"
      "s_memrealtime s[64:65]\n\t"
      "ds_read_b32 %[vf], %[af]\n\t"
      "s_lshl_b32 s66, %[w], 3\n\t"
      "s_add_u32 s70, s62, s66\n\t"
      "s_addc_u32 s71, s63, 0\n\t"
      "s_lshl_b32 s66, %[t], 14\n\t"
      "s_and_b32 m0, s66, 0x1c000\n\t"
      "s_swappc_b64 s[68:69], s[70:71]\n\t"
      DMA4(v0, v1, v2, v3)
      "s_add_u32 m0, m0, 0x1000\n\t"
      "s_swappc_b64 s[68:69], s[70:71]\n\t"
      DMA4(v4, v5, v6, v7)
      "s_add_u32 m0, m0, 0x1000\n\t"
      "s_swappc_b64 s[68:69], s[70:71]\n\t"
      DMA4(v8, v9, v10, v11)
      "s_add_u32 m0, m0, 0x1000\n\t"
      "s_swappc_b64 s[68:69], s[70:71]\n\t"
      DMA4(v12, v13, v14, v15)
      "s_lshl_b32 s66, %[t], 4\n\t"
      "s_add_i32 s66, s66, 12\n\t"
      "s_sub_i32 s66, s66, %[w]\n\t"
      "s_ashr_i32 s66, s66, 4\n\t"
      "s_max_i32 %[pub], %[pub], s66\n\t"
      "v_mov_b32 %[vt], %[pub]\n\t"
      "s_waitcnt lgkmcnt(0)\n\t"
      "v_readlane_b32 s65, %[vf], 63\n\t"
      "s_lshl_b32 s64, s64, 7\n\t"
      "s_sub_i32 s64, s64, %[sch]\n\t"
      "v_mov_b32 %[ve], s64\n\t"
      "s_add_i32 %[sch], %[sch], " STR(TSCHED) "\n\t"
      "ds_write2_b32 %[al], %[vt], %[ve] offset1:64\n\t"
      "s_sub_i32 s64, s64, s65\n\t"
      "s_ashr_i32 s64, s64, 4\n\t"
      "s_mulk_i32 s64, " STR(GAIN) "\n\t"
      "s_ashr_i32 s64, s64, 11\n\t"
      "s_add_i32 s64, s64, " STR(W0) "\n\t"
      "s_max_i32 s64, s64, " STR(WMIN) "\n\t"
      "s_min_i32 %[w], s64, " STR(WMAX) "\n\t"
      "s_and_b32 s66, %[t], 3\n\t"
      "s_movk_i32 s67, 0x800\n\t"
      "s_mov_b32 s72, 0\n\t"
      "s_cmp_eq_u32 s66, 3\n\t"
      "s_cmov_b32 s67, " BIGINC "\n\t"
      BIGHI
      "s_add_u32 s60, s60, s67\n\t"
      "s_addc_u32 s61, s61, s72\n\t"
      "s_add_i32 %[t], %[t], 1\n\t"
      "s_add_i32 s66, %[t], -7\n"
      "2:\n\t"
      "v_cmp_gt_i32 s[64:65], s66, %[vf]\n\t"
      "s_cmp_lg_u32 s64, 0\n\t"
      "s_cbranch_scc1 3f\n"
      "4:\n\t"
      "s_cmp_lg_u32 %[t], %[tend]\n\t"
      "s_cbranch_scc1 1b\n\t"
      "s_branch 8f\n"
      "3:\n\t"
      "s_cmp_eq_u32 s73, 0\n\t"
      "s_cbranch_scc1 4b\n\t"
      "s_sub_u32 s73, s73, 1\n\t"
      "ds_read_b32 %[vf], %[af]\n\t"
      "s_waitcnt lgkmcnt(0)\n\t"
      "s_branch 2b\n"
      "9:\n\t"
      ".set i_, " STR(WMIN) "\n\t.rept " STR(WMAX) "-" STR(WMIN) "+1\n\ts_waitcnt vmcnt(i_)\n\ts_setpc_b64 s[68:69]\n\t.set i_, i_+1\n\t.endr\n"
      "8:"
      : [t] "+s"(t), [w] "+s"(w), [pub] "+s"(pub), [sch] "+s"(sch), [vf] "=&v"(vf), [vt] "=&v"(vt), [ve] "=&v"(ve)
      : [p] "s"(p), [tend] "s"(tend), [af] "v"(a_free), [al] "v"(a_full),
        [v0] "v"(v[0]), [v1] "v"(v[1]), [v2] "v"(v[2]), [v3] "v"(v[3]), [v4] "v"(v[4]), [v5] "v"(v[5]), [v6] "v"(v[6]), [v7] "v"(v[7]),
        [v8] "v"(v[8]), [v9] "v"(v[9]), [v10] "v"(v[10]), [v11] "v"(v[11]), [v12] "v"(v[12]), [v13] "v"(v[13]), [v14] "v"(v[14]), [v15] "v"(v[15])
      : "memory", "scc", "s60", "s61", "s62", "s63", "s64", "s65", "s66", "s67", "s68", "s69", "s70", "s71", "s72", "s73");
}
static_assert(WMAX <= 44 && WMIN >= 13 && WMIN <= W0 && W0 <= WMAX && WMIN <= WSTART && WSTART <= WMAX, "window");
template <int SLEEP> __device__ __forceinline__ unsigned lds_wait(LAS unsigned char* L, int off, unsigned want) {
  unsigned v = 0;
#pragma unroll 1
  for (unsigned s = 0; s < SPIN_CAP; ++s) {
    v = __builtin_amdgcn_readfirstlane(*(volatile LAS unsigned*)(L + off));
    if (v >= want) break;
    if (SLEEP) __builtin_amdgcn_s_sleep(SLEEP);
  }
  asm volatile("" ::: "memory");
  return v;
}
__device__ __forceinline__ int ccol(int n) { return 8 * (n & 7) + 4 * (n >> 3); }
template <int LD, int RS, int RK> __device__ __forceinline__ int tile_voff(int lane) { return ((RS * ((lane >> 3) & 1) + RK * (lane >> 4)) * LD + 8 * (lane & 7)) * 2; }
template <int LD> __device__ __forceinline__ void ldtile(u32x4 (&raw)[8], __amdgpu_buffer_rsrc_t r, int elem0, int voff) {
#pragma unroll
  for (int j = 0; j < 8; ++j) raw[j] = __builtin_amdgcn_raw_buffer_load_b128(r, voff, (elem0 + j * LD) * 2, AUX_NT);
}
__device__ __forceinline__ void pairrows(const u32x4 (&raw)[8], u32x4 (&f)[8]) {
#pragma unroll
  for (int jp = 0; jp < 4; ++jp)
#pragma unroll
    for (int d = 0; d < 4; ++d) {
      f[2 * d][jp] = __builtin_amdgcn_perm(raw[2 * jp + 1][d], raw[2 * jp][d], 0x05040100u);
      f[2 * d + 1][jp] = __builtin_amdgcn_perm(raw[2 * jp + 1][d], raw[2 * jp][d], 0x07060302u);
    }
}
__device__ __forceinline__ void pin8(u32x4 (&f)[8]) {
  asm volatile("" : "+v"(f[0]), "+v"(f[1]), "+v"(f[2]), "+v"(f[3]), "+v"(f[4]), "+v"(f[5]), "+v"(f[6]), "+v"(f[7]));
}
__device__ __forceinline__ void mkfrag(const u32x4 (&raw)[8], u32x4 (&f)[8]) {
  u32x4 p[8];
  pairrows(raw, p);
#pragma unroll
  for (int c = 0; c < 4; ++c)
#pragma unroll
    for (int jp = 0; jp < 4; ++jp) {
      f[c][jp] = __builtin_amdgcn_update_dpp((int)p[c][jp], (int)p[4 + c][jp], ROR8, 0xf, 0xc, false);
      f[4 + c][jp] = __builtin_amdgcn_update_dpp((int)p[4 + c][jp], (int)p[c][jp], ROR8, 0xf, 0x3, false);
    }
  pin8(f);
}
__device__ __forceinline__ void mld4(u32x4& v, u64 m, unsigned voff, const unsigned char* sb) {
  asm volatile("s_mov_b64 exec, %1\n\tglobal_load_dwordx4 %0, %2, %3 sc1\n\ts_mov_b64 exec, -1" : "+v"(v) : "s"(m), "v"(voff), "s"(sb) : "memory");
}
__device__ __forceinline__ void mld2(u32x2& v, u64 m, unsigned voff, const unsigned char* sb) {
  asm volatile("s_mov_b64 exec, %1\n\tglobal_load_dwordx2 %0, %2, %3 sc1\n\ts_mov_b64 exec, -1" : "+v"(v) : "s"(m), "v"(voff), "s"(sb) : "memory");
}
__device__ __forceinline__ u64 inval4(u32x4 v) { return __ballot(max(max(v[0], v[1]), max(v[2], v[3])) == SENT); }
__device__ __forceinline__ u64 inval2(u32x2 v) { return __ballot(max(v[0], v[1]) == SENT); }

#define ST(i) do {} while (0)
#define STY(i) ST(i)

template <typename T> __device__ __forceinline__ void pin(T& v) { asm volatile("" : "+v"(v)); }
typedef short s16x4 __attribute__((ext_vector_type(4)));
__device__ __forceinline__ u32x2 tr_read(LAS unsigned char* p) {
  return __builtin_bit_cast(u32x2, __builtin_amdgcn_ds_read_tr16_b64_v4i16((LAS s16x4*)p));
}
template <int R, int SLP> __device__ __forceinline__ void gather4(u32x4 (&v)[4], unsigned off, const unsigned char* base, int stride) {
  u64 mk[4];
#pragma unroll
  for (int i = 0; i < 4; ++i) mk[i] = inval4(v[i]);
#pragma unroll 1
  for (unsigned spins = 0; spins < SPIN_CAP; ++spins) {
#pragma unroll
    for (int i = 0; i < 4; ++i) mld4(v[i], mk[i], off, base + i * stride);
    if (SLP) __builtin_amdgcn_s_sleep(SLP);
    asm volatile("s_waitcnt vmcnt(%4)" : "+v"(v[0]), "+v"(v[1]), "+v"(v[2]), "+v"(v[3]) : "i"(4 * (R - 1)) : "memory");
#pragma unroll
    for (int i = 0; i < 4; ++i) mk[i] = inval4(v[i]);
    if (((mk[0] | mk[1]) | (mk[2] | mk[3])) == 0) break;
  }
}
template <int R> __device__ __forceinline__ void drain4(u32x4 (&v)[4]) {
  if (R > 1) asm volatile("s_waitcnt vmcnt(0)" : "+v"(v[0]), "+v"(v[1]), "+v"(v[2]), "+v"(v[3])::"memory");
}
__device__ __forceinline__ void warm_code(int lane) {
  u64 pc;
  asm volatile("s_getpc_b64 %0" : "=s"(pc));
  const unsigned t = *(const GAS unsigned*)((pc & ~63ull) + lane * 64);
  asm volatile("" ::"v"(t));
}
#define LP(T, a) ((LAS T*)(size_t)(a))
__device__ __forceinline__ int role_b(int g) {
  const int xc = g & 7, i = g >> 3;
  if (CXCD == 1) return xc < 7 && i < 7 ? xc * 8 + i : (xc == 0 && i < 14 ? 56 + i - 7 : -1);
  if (CXCD == 3) return xc < 6 && i < 7 ? xc * 8 + i : (xc < 2 && i < 14 ? (6 + xc) * 8 + i - 7 : -1);
  if (CXCD == 5) return xc < 4 && i < 14 ? (i < 7 ? xc * 8 + i : (4 + xc) * 8 + i - 7) : -1;
  return i < 7 ? xc * 8 + i : -1;
}
__device__ __forceinline__ int role_c(int g) {
  const int xc = g & 7, i = g >> 3;
  if (CXCD == 1) return xc == 7 && i < 28 ? i : -1;
  if (CXCD == 3) return xc >= 6 && i < 14 ? (xc - 6) * 14 + i : -1;
  if (CXCD == 5) return xc >= 4 && i < 7 ? (xc - 4) * 7 + i : -1;
  if (CXCD == 7) return xc < 7 && i >= 7 && i < 11 ? 4 * xc + i - 7 : -1;
  if (CXCD == 8) return i >= 7 && (i - 7) * 8 + xc < 28 ? (i - 7) * 8 + xc : -1;
  if (CXCD == 2) return xc >= 6 && i >= 7 && i < 21 ? (xc - 6) * 14 + i - 7 : -1;
  return xc >= 4 && i >= 7 && i < 14 ? (xc - 4) * 7 + i - 7 : -1;
}

__global__ void __launch_bounds__(NTHR, 3)
fused_block(const bf16_t* __restrict__ x, const bf16_t* __restrict__ kc, const bf16_t* __restrict__ vc,
            const bf16_t* __restrict__ wq, const bf16_t* __restrict__ wo, const bf16_t* __restrict__ wg,
            const bf16_t* __restrict__ wu, const bf16_t* __restrict__ wd, bf16_t* __restrict__ out, unsigned char* ws) {
  extern __shared__ __attribute__((aligned(16))) unsigned char smem[];
  LAS unsigned char* L = (LAS unsigned char*)smem;
  const int tid = threadIdx.x, lane = tid & 63, wave = __builtin_amdgcn_readfirstlane(tid >> 6);
  const int g = blockIdx.x;
  const int bh = g >> 3;

  if (__builtin_expect(wave == 0, 1)) {
    __builtin_amdgcn_s_setprio(LPRIO);
    const size_t chunk = ((size_t)bh * SK + ((g + bh) & 7) * 2048) * (DH * 2);
    unsigned vo[16];
#pragma unroll
    for (int i = 0; i < 16; ++i) vo[i] = lane * 16 + (i & 7) * 8192 + (i >> 3) * 1024 - (i & 3) * 1024;
    const char* p = (const char*)kc + chunk;
    int t = 0, pub = 0, w = WSTART, sch = 0;
    __builtin_amdgcn_s_barrier();
    asm volatile("" ::: "memory");
#pragma unroll 1
    for (int tend = NPH; tend <= NSTEP; tend += NPH) {
      stream_steps(vo, p, t, tend, w, pub, sch, lane < 32 ? L_FREE + (lane & 3) * 4 : L_CORR, L_FULL + lane * 4);
      p = (const char*)vc + chunk;
#pragma unroll
      for (int i = 0; i < 16; ++i) vo[i] ^= (lane >> 5) << 7;
    }
    volatile LAS int* vfull = (volatile LAS int*)(L + L_FULL) + lane;
    waitv<32>(); *vfull = NSTEP - 2;
    waitv<16>(); *vfull = NSTEP - 1;
    waitv<0>();  *vfull = NSTEP;
    ST(3);
    return;
  }
  if (wave == 1) {
    const unsigned e0 = (unsigned)__builtin_amdgcn_s_memrealtime() << 7;
    ((LAS unsigned*)(L + L_FULL))[lane] = 0u;
    if (lane < 32) {
      ((LAS unsigned*)(L + L_CTL))[lane] = lane == (L_CORR - L_CTL) / 4 ? e0 : 0u;
      ((LAS unsigned*)(L + L_ZERO))[lane] = 0u; ((LAS unsigned*)(L + L_ST))[lane] = 0u;
    }
  }
  bar();
  const __amdgpu_buffer_rsrc_t rs = __builtin_amdgcn_make_buffer_rsrc(ws, 0, W_END, 0x00020000);
  const int n = lane & 15, kg = lane >> 4;
  if (wave <= 4) {
    const int ci = wave - 1;
    unsigned full = 0;
    volatile LAS unsigned* vfree = (volatile LAS unsigned*)(L + L_FREE) + ci;
    {
      const int lo = (8 * ci + kg) * 512 + 16 * n;
      auto fetch = [&](int t, u32x4 (&d)[4]) {
        if ((int)full <= t) full = lds_wait<CSLEEP>(L, L_FULL, t + 1);
        LAS unsigned char* sb = L + (t & (NSLOT - 1)) * SLOT_BYTES + lo;
        d[0] = *(LAS u32x4*)(sb); d[1] = *(LAS u32x4*)(sb + 256); d[2] = *(LAS u32x4*)(sb + 2048); d[3] = *(LAS u32x4*)(sb + 2304);
        waitl();
        *vfree = t + 1;
        asm volatile("" : "+v"(d[0]), "+v"(d[1]), "+v"(d[2]), "+v"(d[3])::"memory");
      };
      u32x4 pre[PRE][4];
#pragma unroll
      for (int t = 0; t < PRE; ++t) fetch(t, pre[t]);
      lds_wait<1>(L, L_QRDY, 1u);
      const u32x4 q0 = *(LAS u32x4*)(L + L_Q + 16 * n), q1 = *(LAS u32x4*)(L + L_Q + 256 + 16 * n);
      constexpr float C2 = 0.0625f * 1.4426950408889634f;
      float m = -INFINITY;
      LAS float* sp = (LAS float*)(L + L_P) + 8 * ci + kg + 4 * (n & 1);
      auto score = [&](int t, const u32x4 (&k)[4]) {
        float s0 = 0.f, s1 = 0.f;
#pragma unroll
        for (int j = 0; j < 4; ++j) { s0 = dot2(k[0][j], q0[j], s0); s1 = dot2(k[2][j], q0[j], s1); }
#pragma unroll
        for (int j = 0; j < 4; ++j) { s0 = dot2(k[1][j], q1[j], s0); s1 = dot2(k[3][j], q1[j], s1); }
        s0 += dpp<XOR1>(s0); s1 += dpp<XOR1>(s1);
        float s = (n & 1) ? s1 : s0;
        s += dpp<XOR2>(s); s += dpp<ROR4>(s); s += dpp<ROR8>(s);
        s = rbf(s) * C2;
        m = fmaxf(m, s);
        if (n < 2) sp[t * 32] = s;
      };
#pragma unroll
      for (int t = 0; t < PRE; ++t) score(t, pre[t]);
#pragma unroll 1
      for (int t = PRE; t < NPH; ++t) { u32x4 k[4]; fetch(t, k); score(t, k); }
      m = fmaxf(m, dpp<XOR1>(m));
      m = x32_max(x16_max(m));
      if (lane == 0) ((LAS float*)(L + L_MW))[ci] = m;
      waitl();
      if (lane == 0) __hip_atomic_fetch_add((LAS unsigned*)(L + L_C1), 1u, RLX_WG);
      lds_wait<0>(L, L_C1, 4u);
      const f32x4 mw = *(LAS f32x4*)(L + L_MW);
      const float M = fmaxf(fmaxf(mw[0], mw[1]), fmaxf(mw[2], mw[3]));
      LAS unsigned char* pp = L + L_P + (ci * 64 + lane) * 32;
      const f32x4 e0 = *(LAS f32x4*)pp, e1 = *(LAS f32x4*)(pp + 16);
      u32x4 hi, lo2;
      float l = 0.f;
#pragma unroll
      for (int j = 0; j < 4; ++j) {
        const float a = __builtin_amdgcn_exp2f((j < 2 ? e0[2 * j] : e1[2 * j - 4]) - M), b = __builtin_amdgcn_exp2f((j < 2 ? e0[2 * j + 1] : e1[2 * j - 3]) - M);
        l += a + b;
        hi[j] = pk2(a, b);
        lo2[j] = pk2(a - bflo(hi[j]), b - bfhi(hi[j]));
      }
      *(LAS u32x4*)pp = hi; *(LAS u32x4*)(pp + 16) = lo2;
      l = wave_sum(l);
      if (lane == 0) ((LAS float*)(L + L_LW))[ci] = l;
      waitl();
      if (lane == 0) __hip_atomic_fetch_add((LAS unsigned*)(L + L_C2), 1u, RLX_WG);
      lds_wait<0>(L, L_C2, 4u);
    }
    {
      const int q = n >> 2;
      const int lo = q * 512 + (ci ^ (q & 1)) * 128 + kg * 32 + (n & 3) * 8;
      const bool act = q == kg && (n & 3) < 2;
      unsigned pa = act ? L_P + (n & 1) * 16 : L_ZERO;
      const unsigned pinc = act ? 128u : 0u;
      int off = (lane & 1) ? OOB : W_RECA + g * RECA + (64 * ci + lane) * 2;
      pin(off);
      f32x4 acc = {0.f, 0.f, 0.f, 0.f};
      float sc = 0.f;
#pragma unroll 1
      for (int t = NPH; t < NSTEP; ++t) {
        u32x4 a[4];
#pragma unroll
        for (int m = 0; m < 4; ++m) a[m] = *(LAS u32x4*)(L + pa + 32 * m);
        pa += pinc;
        if (t == NSTEP - 4) { lds_wait<1>(L, L_SCRDY, 1u); sc = *(LAS float*)(L + L_SCALE); }
        if ((int)full <= t) full = lds_wait<VSLEEP>(L, L_FULL, t + 1);
        LAS unsigned char* sb = L + (t & (NSLOT - 1)) * SLOT_BYTES + lo;
        u32x2 v[8];
#pragma unroll
        for (int j = 0; j < 8; ++j) v[j] = tr_read(sb + 2048 * j);
#pragma unroll
        for (int m = 0; m < 4; ++m) acc = mfma(a[m], u32x4{v[2 * m][0], v[2 * m][1], v[2 * m + 1][0], v[2 * m + 1][1]}, acc);
        waitl();
        *vfree = t + 1;
      }
      if (wave == 1) ST(4);
      const float o = (acc[0] + acc[1]) * sc;
      __builtin_amdgcn_raw_buffer_store_b32(canonu(pk2(o, dpp<XOR1>(o))), rs, off, 0, AUX_SC1);
      if (lane == 0) __hip_atomic_fetch_add((LAS unsigned*)(L + L_PUB), 1u, RLX_WG);
      if (wave == 1) ST(5);
    }
    return;
  }
  const int tw = wave - 5;
  const int rb = role_b(g), jc = role_c(g);
  const int ks = rb >> 3, cs = rb & 7;
  u64 tag;
  {
    const int hq = 2 * (g & 7) + (g >> 7), bq = (g >> 3) & 1, cq = (g >> 4) & 7;
    u32x4 wqr[4], xall;
    unsigned xr[4];
    const int row0 = 64 * tw + (lane >> 2);
    xall = *(const GAS u32x4*)(x + bq * D + 8 * min(lane, 55));
#pragma unroll
    for (int i = 0; i < 4; ++i) xr[i] = *(const GAS unsigned short*)(x + bq * D + row0 + 16 * i);
#pragma unroll
    for (int i = 0; i < 4; ++i)
      wqr[i] = __builtin_nontemporal_load((const GAS u32x4*)(wq + (size_t)(row0 + 16 * i) * HD + hq * DH + 32 * cq + 8 * (lane & 3)));
    unsigned z = 0;
    pin(z);
    const u64 gen = __hip_atomic_load((GAS u64*)(ws + W_GEN + z), RLX_AGENT);
    __builtin_amdgcn_sched_barrier(0);
    if (lane >= 56) xall = u32x4{0u, 0u, 0u, 0u};
    float xs[8]; float s = 0.f;
#pragma unroll
    for (int i = 0; i < 4; ++i) { xs[2 * i] = bflo(xall[i]); xs[2 * i + 1] = bfhi(xall[i]); s += xs[2 * i] + xs[2 * i + 1]; }
    const float mean = wave_sum(s) * (1.f / D);
    float qv = 0.f;
#pragma unroll
    for (int i = 0; i < 8; ++i) { const float d = xs[i] - mean; qv = fmaf(d, d, qv); }
    if (lane >= 56) qv = 0.f;
    const float var = wave_sum(qv) * (1.f / D);
    const float mb = rbf(mean);
    const float rsd = __builtin_amdgcn_rcpf(rbf(__builtin_amdgcn_sqrtf(rbf(rbf(var) + 1e-5f))));
    float a[8];
#pragma unroll
    for (int j = 0; j < 8; ++j) a[j] = 0.f;
#pragma unroll
    for (int i = 0; i < 4; ++i) {
      const float xn = rbf(rbf(bflo(xr[i]) - mb) * rsd);
#pragma unroll
      for (int j = 0; j < 4; ++j) { a[2 * j] = fmaf(xn, bflo(wqr[i][j]), a[2 * j]); a[2 * j + 1] = fmaf(xn, bfhi(wqr[i][j]), a[2 * j + 1]); }
    }
    float b4[4], b2[2];
#pragma unroll
    for (int j = 0; j < 4; ++j) b4[j] = tr32(a[j], a[j + 4]);
#pragma unroll
    for (int j = 0; j < 2; ++j) { b2[j] = tr16(b4[j], b4[j + 2]); b2[j] += dpp<ROR4>(b2[j]); b2[j] += dpp<ROR8>(b2[j]); }
    if ((lane & 12) == 0) {
      const int col = 8 * (lane & 3) + 4 * (lane >> 5) + 2 * ((lane >> 4) & 1);
      *(LAS f32x2*)(L + L_QPART + (tw * 32 + col) * 4) = f32x2{b2[0], b2[1]};
    }
    waitl();
    if (lane == 0) __hip_atomic_fetch_add((LAS unsigned*)(L + L_QCNT), 1u, RLX_WG);
    tag = mix64(((u64)__builtin_amdgcn_readfirstlane((unsigned)(gen >> 32)) << 32) | __builtin_amdgcn_readfirstlane((unsigned)gen));
    if (tw == 0) {
      lds_wait<0>(L, L_QCNT, 7u);
      ST(1);
      const u64 t48 = tag & ~0xFFFFull;
      if (lane < 32) {
        float qs = 0.f;
#pragma unroll
        for (int w = 0; w < 7; ++w) qs += ((LAS float*)(L + L_QPART))[w * 32 + lane];
        __hip_atomic_store((GAS u64*)(ws + W_QG) + bq * HD + hq * DH + 32 * cq + lane, t48 | f2bf(qs), RLX_AGENT);
      }
      GAS u64* qg = (GAS u64*)(ws + W_QG) + bh * DH + lane;
      u64 v[4];
#pragma unroll 1
      for (unsigned spins = 0; spins < SPIN_CAP; ++spins) {
        bool ok = true;
#pragma unroll
        for (int i = 0; i < 4; ++i) v[i] = __hip_atomic_load(qg + 64 * i, RLX_AGENT);
#pragma unroll
        for (int i = 0; i < 4; ++i) ok &= (v[i] & ~0xFFFFull) == t48;
        if (__all(ok)) break;
      }
#pragma unroll
      for (int i = 0; i < 4; ++i) ((LAS unsigned short*)(L + L_Q))[64 * i + lane] = (unsigned short)v[i];
      waitl();
      *(volatile LAS unsigned*)(L + L_QRDY) = 1u;
      ST(2);
    }
  }
  auto exchange = [&]() {
    lds_wait<8>(L, L_C2, 4u);
    const u32x4 sv = {SENT, SENT, SENT, SENT};
    __builtin_amdgcn_raw_buffer_store_b128(sv, rs, lane < 32 ? W_RECA + g * RECA + 16 * lane : OOB, 0, AUX_SC1);
    if (rb >= 0) __builtin_amdgcn_raw_buffer_store_b128(sv, rs, lane < 32 ? W_RECB + ((ks * 2 + kg) * D + 64 * cs + 4 * n) * 4 : OOB, 0, AUX_SC1);
    if (jc >= 0) {
#pragma unroll
      for (int i = 0; i < 4; ++i) __builtin_amdgcn_raw_buffer_store_b128(sv, rs, lane < 56 ? W_RECC + jc * (2 * D * 4) + (56 * i + lane) * 16 : OOB, 0, AUX_SC1);
    }
    const f32x4 mw = *(LAS f32x4*)(L + L_MW), lw = *(LAS f32x4*)(L + L_LW);
    const float Mw = fmaxf(fmaxf(mw[0], mw[1]), fmaxf(mw[2], mw[3])), Lw = (lw[0] + lw[1]) + (lw[2] + lw[3]);
    __builtin_amdgcn_raw_buffer_store_b64(u32x2{__float_as_uint(Mw), __float_as_uint(Lw)}, rs, lane == 0 ? W_ML + g * 8 : OOB, 0, AUX_SC1);
    waitv<0>();
    if (lane == 0) __hip_atomic_store((GAS u64*)(ws + W_FLAG) + g, tag, RLX_AGENT);
    lds_wait<16>(L, L_FULL, ML_STEP);
    const bool all = rb >= 0 || jc >= 0;
    GAS u64* fp = (GAS u64*)(ws + W_FLAG) + (all ? lane : 8 * bh + (lane & 7));
    const int fs = all ? 64 : 0;
#pragma unroll 1
    for (unsigned spins = 0; spins < SPIN_CAP; ++spins) {
      bool ok = true;
#pragma unroll
      for (int i = 0; i < 4; ++i) ok &= __hip_atomic_load(fp + fs * i, RLX_AGENT) == tag;
      if (__all(ok)) break;
      __builtin_amdgcn_s_sleep(16);
    }
    asm volatile("" ::: "memory");
    const u64 ml = __hip_atomic_load((GAS u64*)(ws + W_ML) + 8 * bh + (lane & 7), RLX_AGENT);
    const float mi = __uint_as_float((unsigned)ml), li = __uint_as_float((unsigned)(ml >> 32));
    float Mg = fmaxf(mi, dpp<XOR1>(mi)); Mg = fmaxf(Mg, dpp<XOR2>(Mg)); Mg = fmaxf(Mg, dpp<HMIR>(Mg));
    float Lg = li * __builtin_amdgcn_exp2f(mi - Mg);
    Lg += dpp<XOR1>(Lg); Lg += dpp<XOR2>(Lg); Lg += dpp<HMIR>(Lg);
    *(volatile LAS float*)(L + L_SCALE) = __builtin_amdgcn_exp2f(Mw - Mg) / Lg;
    waitl();
    *(volatile LAS unsigned*)(L + L_SCRDY) = 1u;
    if (g == 0 && lane == 0) __hip_atomic_store((GAS u64*)(ws + W_GEN), tag, RLX_AGENT);
  };
  if (tw == 5) *(volatile LAS unsigned*)(L + L_CORR) += LEAD * 128;
  auto pace = [&]() {
    const unsigned tag32 = (unsigned)(tag >> 32);
    unsigned A = __builtin_amdgcn_readfirstlane(*(volatile LAS unsigned*)(L + L_CORR)), prev = 0;
    int Bd = 0, tk = 0, f = 0;
    const int adv = rb >= 0 ? ADVB : jc >= 0 ? ADVC : 0;
    auto run_until = [&](int tt) {
#pragma unroll 1
      while (f < tt) {
        const int nf = (int)lds_wait<8>(L, L_FULL, f + 1);
        f = max(nf, f + 1);
        *(volatile LAS unsigned*)(L + L_CORR) = A + Bd * (f - tk) - adv * f;
      }
    };
#pragma unroll 1
    for (int k = 0; k < NCP; ++k) {
      const int tc = CP0 + CPS * k;
      run_until(tc);
      const unsigned e = __builtin_amdgcn_readfirstlane(*(volatile LAS unsigned*)(L + L_E));
      if (lane == 0 && (g >> 5) == 7) __hip_atomic_store((GAS u64*)(ws + W_PROG) + k * 256 + (g & 31), ((u64)tag32 << 32) | e, RLX_AGENT);
      run_until(tc + CPD + (STAG ? (g >> 3) % STAG : 0));
      float sum = 0.f, cnt = 0.f;
      {
        const u64 pv = __hip_atomic_load((GAS u64*)(ws + W_PROG) + k * 256 + (lane & 31), RLX_AGENT);
        const bool ok = (unsigned)(pv >> 32) == tag32;
        sum += ok ? (float)(int)((unsigned)pv - e) : 0.f; cnt += ok ? 1.f : 0.f;
      }
      sum = wave_sum(sum); cnt = wave_sum(cnt);
      const unsigned eb = e + __builtin_amdgcn_readfirstlane((int)(sum / fmaxf(cnt, 1.f)));
      if (k) Bd = (int)(eb - prev) / CPS - BIAS;
      prev = eb; A = eb; tk = tc;
    }
    run_until(NSTEP - 6);
  };
  if (jc < 0) {
    if (tw == 5) pace();
    if (tw == 4) exchange();
  }
  auto wait_weights = [&](int step) {
    if (step > NSTEP) { lds_wait<8>(L, L_FULL, NSTEP - 2); lds_wait<1>(L, L_PUB, 4u); } else lds_wait<TWSLEEP>(L, L_FULL, step);
  };
  if (jc < 0) {
    if (rb < 0) {
      if (PF_STEP && bh >= 11) {
        const int xc = g & 7;
        const GAS char* wo_ = (const GAS char*)wo + xc * (512 * D * 2) + lane * 16;
        const GAS char* wd_ = (const GAS char*)wd + min(xc, 6) * (256 * D * 2) + lane * 16;
        const int og = (lane >> 5) * (FF * 2) + min(xc, 6) * 512 + (lane & 31) * 16;
        wait_weights(PF_STEP);
        u32x4 t[8];
#pragma unroll
        for (int r = 0; r < (TWB > NSTEP - 8 ? 8 : 5); ++r) {
          int pc = r * 147 + (bh - 11) * 7 + tw + (TWB > NSTEP - 8 ? 0 : 448);
          if (pc >= 1120 || (xc == 7 && pc >= 448)) pc = 0;
          const GAS char* a = pc < 448 ? wo_ + pc * 1024 : pc < 672 ? (const GAS char*)wg + og + (pc - 448) * (4 * FF)
                              : pc < 896 ? (const GAS char*)wu + og + (pc - 672) * (4 * FF) : wd_ + (pc - 896) * 1024;
          t[r] = __builtin_nontemporal_load((const GAS u32x4*)a);
        }
#pragma unroll
        for (int r = 0; r < (TWB > NSTEP - 8 ? 8 : 5); ++r) asm volatile("" ::"v"(t[r]));
      }
      return;
    }
    if (tw >= 4) return;
    wait_weights(TWB + TWS * tw);
    u32x4 fr[2][8];
    {
      const __amdgpu_buffer_rsrc_t r = __builtin_amdgcn_make_buffer_rsrc((void*)wo, 0, HD * D * 2, 0x00020000);
      u32x4 raw[2][8];
      const int voff = tile_voff<D, 32, 8>(lane);
#pragma unroll
      for (int s = 0; s < 2; ++s) ldtile<D>(raw[s], r, (512 * ks + 128 * tw + 64 * s) * D + 64 * cs, voff);
#pragma unroll
      for (int s = 0; s < 2; ++s) mkfrag(raw[s], fr[s]);
    }
    unsigned off = W_RECA + (((n & 1) * 16 + 2 * ks + (tw >> 1)) * 8 + (n >> 1)) * RECA + (128 * (tw & 1) + 8 * kg) * 2;
    unsigned wa = L_RED + ((tw * 2 + (lane >> 5)) * 64 + ccol(n) + 2 * (kg & 1)) * 4, ra = L_RED + lane * 8;
    int so = W_RECB + ((ks * 2 + (lane >> 5)) * D + 64 * cs + 2 * (lane & 31)) * 4;
    u32x4 v[4];
#pragma unroll
    for (int i = 0; i < 4; ++i) { v[i] = u32x4{SENT, SENT, SENT, SENT}; pin(v[i]); }
    pin(off); pin(wa); pin(ra); pin(so);
    if (tw == 0) ST(14);
    lds_wait<4>(L, L_SCRDY, 1u);
    if (WARMC && tw == 3) { lds_wait<8>(L, L_FULL, WARMC); warm_code(lane); }
    lds_wait<1>(L, L_FULL, NSTEP);
    if (BDELAY) __builtin_amdgcn_s_sleep(BDELAY);
    gather4<RB, SB>(v, off, ws, 64);
    if (tw == 0) ST(6);
    f32x4 d[4];
#pragma unroll
    for (int c = 0; c < 4; ++c) d[c] = mfma(v[0], fr[0][c], f32x4{0.f, 0.f, 0.f, 0.f});
#pragma unroll
    for (int s = 1; s < 4; ++s)
#pragma unroll
      for (int c = 0; c < 4; ++c) d[c] = mfma(v[s], fr[s >> 1][4 * (s & 1) + c], d[c]);
    float e[4];
#pragma unroll
    for (int c = 0; c < 4; ++c) e[c] = tr32(d[c][0] + d[c][2], d[c][1] + d[c][3]);
    *LP(f32x2, wa) = f32x2{tr16(e[0], e[2]), tr16(e[1], e[3])};
    bar();
    if (tw == 0) {
      const f32x2 s = (*LP(f32x2, ra) + *LP(f32x2, ra + 512)) + (*LP(f32x2, ra + 1024) + *LP(f32x2, ra + 1536));
      __builtin_amdgcn_raw_buffer_store_b64(u32x2{canon(s[0]), canon(s[1])}, rs, so, 0, AUX_SC1);
      ST(7);
    }
    drain4<RB>(v);
    return;
  }
#define TS(i) do {} while (0)
  u32x4 fg[2][8], fd[8];
  unsigned tpub = 0;
  {
    const GAS char* gp = (const GAS char*)((n & 8) ? wu : wg) + ((64 * tw + 16 * kg) * FF + 64 * jc + 8 * (n & 7)) * 2;
    const __amdgpu_buffer_rsrc_t r3 = __builtin_amdgcn_make_buffer_rsrc((void*)wd, 0, FF * D * 2, 0x00020000);
    wait_weights(tw == 5 ? 6 : TWC + TWS * tw);
    u32x4 raw[2][8];
#pragma unroll
    for (int j = 0; j < 16; ++j) raw[j >> 3][j & 7] = __builtin_nontemporal_load((const GAS u32x4*)(gp + j * (FF * 2)));
    __builtin_amdgcn_sched_barrier(0);
    pairrows(raw[0], fg[0]); pin8(fg[0]);
    pairrows(raw[1], fg[1]); pin8(fg[1]);
    if (!WDLATE) { ldtile<D>(raw[0], r3, 64 * jc * D + 64 * tw, tile_voff<D, 32, 8>(lane)); mkfrag(raw[0], fd); }
    if (tw == 5) pace();
    if (tw == 4) exchange();
    if (WARMC && tw == 3) { lds_wait<8>(L, L_FULL, WARMC); warm_code(lane); warm_code(lane + 64); }
    if (WDLATE) { wait_weights(NSTEP + 1); tpub = (unsigned)__builtin_amdgcn_s_memrealtime(); ldtile<D>(raw[0], r3, 64 * jc * D + 64 * tw, tile_voff<D, 32, 8>(lane)); mkfrag(raw[0], fd); }
  }
  if (tw == 0) ST(15);
  const int b = n & 1;
  const int col = 64 * tw + 16 * kg + 4 * (n >> 2);
  const u32x2 xp = *(const GAS u32x2*)(x + b * D + col);
  f32x4 xf = {bflo(xp[0]), bfhi(xp[0]), bflo(xp[1]), bfhi(xp[1])};
  unsigned off = W_RECB + (((n & 2) * 4 + b) * D + col) * 4;
  unsigned a_h = L_H + (b * D + col) * 2, a_st = L_ST + (b * 8 + tw) * 8 + (lane >> 5) * 4;
  unsigned a_red = L_RED + tw * 1536 + (n == 4 ? 2 : b) * 512 + (kg >> 1) * 256 + (kg & 1) * 16;
  unsigned a_rd = L_RED + tw * 512 + lane * 4, a_rc = L_RED + 1024 + lane * 4, a_sr = L_ST + (tw * 8 + (lane & 7)) * 8;
  unsigned a_g = L_G + (tw * 64 + 8 * (lane & 7) + (lane >> 3)) * 2, a_gr = L_G + (b * 64 + 8 * kg) * 2;
  int so = lane < 32 ? W_RECC + ((jc * 2 + kg) * D + 64 * tw + ccol(n)) * 4 : OOB;
  if (!((0x13 >> n) & 1)) { a_red = tw * 2048 + lane * 16; a_st = 16384 + tw * 256 + lane * 4; }
  unsigned msel = (lane & 16) ? ~0u : 0u;
  u64 wr = __ballot((0x13 >> n) & 1);
  const int pg = lane >> 3, ch = lane & 7;
  unsigned offd = W_RECC + pg * (2 * D * 4) + (32 * jc + 4 * ch) * 4, a_hd = L_H + (32 * jc + 4 * ch + 2 * (lane >> 5)) * 2;
  int oo = (lane & 24) ? OOB : (32 * jc + 4 * ch + 2 * (lane >> 5)) * 2;
  u32x4 v[4], w[4], b0, b1;
  constexpr unsigned ONE2 = 0x3F803F80u;
  b0 = u32x4{ONE2, ONE2, ONE2, ONE2}; b1 = b0;
#pragma unroll
  for (int i = 0; i < 4; ++i) {
    v[i] = u32x4{SENT, SENT, SENT, SENT}; pin(v[i]);
    const unsigned iv = (i < 3 || pg < 4) ? SENT : 0u;
    w[i] = u32x4{iv, iv, iv, iv}; pin(w[i]);
  }
  pin(xf); pin(off); pin(a_h); pin(a_st); pin(a_red); pin(a_rd); pin(a_sr); pin(a_g); pin(a_gr); pin(so); pin(offd); pin(a_hd); pin(oo);
  pin(a_rc); pin(msel); pin(b0); pin(b1);
  const __amdgpu_buffer_rsrc_t ro = __builtin_amdgcn_make_buffer_rsrc((void*)out, 0, 2 * D * 2, 0x00020000);
  lds_wait<4>(L, L_SCRDY, 1u);
  lds_wait<1>(L, L_FULL, NSTEP);
  if (CWAIT) {
#pragma unroll 1
    while ((unsigned)__builtin_amdgcn_s_memrealtime() - tpub < (unsigned)CWAIT) __builtin_amdgcn_s_sleep(1);
  }
  gather4<RC, SC>(v, off, ws, 2 * D * 4);
  TS(5);
  if (tw == 0) ST(8);
  {
    f32x4 a = (__builtin_bit_cast(f32x4, v[0]) + __builtin_bit_cast(f32x4, v[1])) + (__builtin_bit_cast(f32x4, v[2]) + __builtin_bit_cast(f32x4, v[3]));
#pragma unroll
    for (int j = 0; j < 4; ++j) { float t = a[j] + dpp<XOR2>(a[j]); pin(t); a[j] = t; }
    const unsigned a01 = pk2(a[0], a[1]), a23 = pk2(a[2], a[3]);
    const unsigned h0 = pk2(xf[0] + bflo(a01), xf[1] + bfhi(a01)), h1 = pk2(xf[2] + bflo(a23), xf[3] + bfhi(a23));
#define ACT(o, v, ctrl) o = (unsigned)__builtin_amdgcn_update_dpp((int)(o), (int)(v), ctrl, 0xf, 0x1, false)
    ACT(b0[0], h0, 0xE4); ACT(b0[1], h1, 0xE4); ACT(b0[2], h0, SHL + 4); ACT(b0[3], h1, SHL + 4);
    ACT(b1[0], h0, SHL + 8); ACT(b1[1], h1, SHL + 8); ACT(b1[2], h0, SHL + 12); ACT(b1[3], h1, SHL + 12);
    f32x4 d[8];
#pragma unroll
    for (int c = 0; c < 8; ++c) {
      d[c] = mfma(fg[1][c], b1, mfma(fg[0][c], b0, f32x4{0.f, 0.f, 0.f, 0.f}));
      if (c) *LP(f32x4, a_red + (c - 1) * 32) = d[c - 1];
      __builtin_amdgcn_sched_group_barrier(0x8, 2, 0); __builtin_amdgcn_sched_group_barrier(0x2, 3, 0); __builtin_amdgcn_sched_group_barrier(0x200, 1, 0);
    }
    *LP(f32x4, a_red + 7 * 32) = d[7];
    float s1 = dot2(h1, ONE2, dot2(h0, ONE2, 0.f)), s2 = dot2(h1, h1, dot2(h0, h0, 0.f));
    s1 = x16_sum(tr32(s1, s2));
    s1 += dpp<ROR4>(s1); s1 += dpp<ROR8>(s1);
    *LP(u32x2, a_h) = u32x2{h0, h1};
    *LP(float, a_st) = s1;
  }
  TS(6);
  bar();
  TS(7);
  if (tw == 0) ST(12);
  if (tw < 2) {
    f32x2 st = *LP(f32x2, a_sr);
    f32x2 ps[7], pc[7];
#pragma unroll
    for (int i = 0; i < 7; ++i) {
      ps[i] = f32x2{*LP(float, a_rd + i * 1536), *LP(float, a_rd + i * 1536 + 256)};
      pc[i] = f32x2{*LP(float, a_rc + i * 1536), *LP(float, a_rc + i * 1536 + 256)};
    }
    __builtin_amdgcn_sched_barrier(0);
#pragma unroll
    for (int j = 0; j < 2; ++j) { st[j] += dpp<XOR1>(st[j]); st[j] += dpp<XOR2>(st[j]); st[j] += dpp<HMIR>(st[j]); }
    const float mean = st[0] * (1.f / D);
    const float var = fmaxf(fmaf(-mean, mean, st[1] * (1.f / D)), 0.f);
    const float mb = rbf(mean);
    const float rsd = __builtin_amdgcn_rcpf(rbf(__builtin_amdgcn_sqrtf(rbf(rbf(var) + 1e-5f))));
    const f32x2 sm = ((ps[0] + ps[1]) + (ps[2] + ps[3])) + ((ps[4] + ps[5]) + ps[6]);
    const f32x2 cm = ((pc[0] + pc[1]) + (pc[2] + pc[3])) + ((pc[4] + pc[5]) + pc[6]);
    const unsigned gu = pk2((sm[0] - mb * cm[0]) * rsd, (sm[1] - mb * cm[1]) * rsd);
    const float xg = bflo(gu);
    constexpr float K1 = -2.f * 0.7978845608028654f * 1.4426950408889634f;
    const float ge = xg * __builtin_amdgcn_rcpf(1.f + __builtin_amdgcn_exp2f(xg * fmaf(xg * xg, K1 * 0.044715f, K1)));
    *LP(unsigned short, a_g) = (unsigned short)f2bf(rbf(ge) * bfhi(gu));
  }
  drain4<RC>(v);
  bar();
  if (tw == 0) ST(13);
  {
    const u32x4 a0 = *LP(u32x4, a_gr), a1 = *LP(u32x4, a_gr + 64);
    f32x4 d[4];
#pragma unroll
    for (int c = 0; c < 4; ++c) d[c] = mfma(a0, fd[c], f32x4{0.f, 0.f, 0.f, 0.f});
#pragma unroll
    for (int c = 0; c < 4; ++c) d[c] = mfma(a1, fd[4 + c], d[c]);
    u32x4 o;
#pragma unroll
    for (int c = 0; c < 4; ++c) o[c] = canonu((__float_as_uint(d[c][1]) & msel) | (__float_as_uint(d[c][0]) & ~msel));
    __builtin_amdgcn_raw_buffer_store_b128(o, rs, so, 0, AUX_SC1);
    if (tw == 0) ST(9);
  }
  if (tw == 6) {
    const unsigned hv = *LP(unsigned, a_hd);
    if (DDELAY) __builtin_amdgcn_s_sleep(DDELAY);
    gather4<RD, SD>(w, offd, ws, 8 * (2 * D * 4));
    ST(10);
    const f32x4 t = (__builtin_bit_cast(f32x4, w[0]) + __builtin_bit_cast(f32x4, w[1])) + (__builtin_bit_cast(f32x4, w[2]) + __builtin_bit_cast(f32x4, w[3]));
    float s0 = tr32(t[0], t[2]), s1 = tr32(t[1], t[3]);
    s0 = x16_sum(s0); s1 = x16_sum(s1);
    s0 += dpp<ROR8>(s0); s1 += dpp<ROR8>(s1);
    const unsigned dn = pk2(s0, s1);
    __builtin_amdgcn_raw_buffer_store_b32(pk2(bflo(hv) + bflo(dn), bfhi(hv) + bfhi(dn)), ro, oo, 0, 0);
    drain4<RD>(w);
    ST(11);
  }
}

extern "C" void kernel_launch(void* const* d_in, const int* in_sizes, int n_in, void* d_out, int out_size, void* d_ws,
                              size_t ws_size, hipStream_t stream) {
  (void)hipFuncSetAttribute((const void*)fused_block, hipFuncAttributeMaxDynamicSharedMemorySize, LDS_BYTES);
  hipLaunchKernelGGL(fused_block, dim3(NWG), dim3(NTHR), LDS_BYTES, stream, (const bf16_t*)d_in[0], (const bf16_t*)d_in[1],
                     (const bf16_t*)d_in[2], (const bf16_t*)d_in[3], (const bf16_t*)d_in[4], (const bf16_t*)d_in[6],
                     (const bf16_t*)d_in[7], (const bf16_t*)d_in[8], (bf16_t*)d_out, (unsigned char*)d_ws);
}
```

```cpp
#include <hip/hip_runtime.h>
#include <hip/hip_bf16.h>
#include <stdint.h>
#include <type_traits>

typedef unsigned short bf16_t;
typedef unsigned long long u64;
typedef unsigned u32x4 __attribute__((ext_vector_type(4)));
typedef unsigned u32x2 __attribute__((ext_vector_type(2)));
typedef float f32x4 __attribute__((ext_vector_type(4)));
typedef float f32x2 __attribute__((ext_vector_type(2)));
typedef __bf16 bf16x2 __attribute__((ext_vector_type(2)));
typedef __bf16 bf16x8 __attribute__((ext_vector_type(8)));
#define LAS __attribute__((address_space(3)))
#define GAS __attribute__((address_space(1)))
#define RLX_AGENT __ATOMIC_RELAXED, __HIP_MEMORY_SCOPE_AGENT
#define RLX_WG __ATOMIC_RELAXED, __HIP_MEMORY_SCOPE_WORKGROUP

constexpr int D = 448, SK = 16384, DH = 256, HD = 4096, FF = 1792;
constexpr int NWG = 256, NTHR = 768;
constexpr int NPH = 64, NSTEP = 2 * NPH;
constexpr int NSLOT = 8, SLOT_BYTES = 16384;
#define W0 19
#define WMIN 15
#define WMAX 32
#define TSCHED 6940
#define LEAD 0
#define NCP 4
#define CP0 14
#define CPS 30
#define CPD 6
#define PMODE 1
#define STAG 8
#define PF_STEP 0
#define CWDELAY 0
#define TWSLEEP 2
#define RB 1
#define SB 0
#define RC 1
#define SC 0
#define RD 1
#define SD 0
#define WDLATE 1
#define JUNK 1
#define CWAIT 0
#define WARMC 120
#define EARLYT 1
#define VTOUCH 0
#define BLO -1000
#define BHI 1000
#define NPRO 2
#define QFIRST 1
#define PAST 300
#define CODEPF 0
#define QPOLL 5
#define QSLEEP 16
#define LEADAT 3
#define FASTPRO 1
#define QLAG 150
#define MAPX 0
#define TLBPF 0
#define TLBAHEAD 10
#define EARLYINV 0
#define WSEND 0
#define INVAT 0
#define XSTAG 0
#define RSTAG 0
#define CX 245
#define BX 140
#define BIAS 40
#define WSTART 28
#define ADVB 0
#define ADVC 40
#define GAIN 8
#define PRE 8
#define TWB 24
#define TWC 36
#define TWS 0
#define ML_STEP 96
#define LPRIO 3
#define CSLEEP 1
#define VSLEEP 1
#define BDELAY 0
#define CDELAY 0
#define DDELAY 6
#define CXCD 7
constexpr int L_CTL = NSLOT * SLOT_BYTES;
constexpr int L_FREE = L_CTL, L_QCNT = L_CTL + 20, L_QRDY = L_CTL + 24, L_C1 = L_CTL + 28, L_C2 = L_CTL + 32,
              L_SCRDY = L_CTL + 36, L_SCALE = L_CTL + 40, L_PUB = L_CTL + 44, L_CORR = L_CTL + 48, L_TFIN = L_CTL + 52, L_MW = L_CTL + 64, L_LW = L_CTL + 80;
constexpr int L_FULL = L_CTL + 128;
constexpr int L_E = L_FULL + 256;
constexpr int L_QX = L_E + 256;
constexpr int L_Q = L_QX;
constexpr int L_P = L_Q + 512;
constexpr int L_ZERO = L_P + 8192;
constexpr int L_RED = L_ZERO + 128;
constexpr int L_QPART = L_RED + 1024;
constexpr int L_ST = L_RED + 10752;
constexpr int L_H = L_ST + 128;
constexpr int L_G = L_H + 1792;
constexpr int L_DBG = L_G + 256;
constexpr int LDS_BYTES = L_DBG + 256;
static_assert(LDS_BYTES <= 160 * 1024, "LDS");
constexpr int W_GEN = 0;
constexpr int W_FLAG = 4096;
constexpr int W_ML = 8192;
constexpr int W_PROG = 12288;
constexpr int W_QG = W_PROG + 16384;
constexpr int W_RECA = W_QG + 65536, RECA = 512;
constexpr int W_RECB = W_RECA + 256 * RECA;
constexpr int W_RECC = W_RECB + 8 * 2 * D * 4;
constexpr int W_END = W_RECC + 28 * 2 * D * 4;
constexpr long long W_STAMP = (1 << 22) - (WSEND ? (1ll << 30) - (8 << 20) : 0);
constexpr unsigned SENT = 0xFFFFFFFFu;
constexpr int OOB = 0x40000000;
constexpr int AUX_SC1 = 16, AUX_NT = 2;
constexpr unsigned SPIN_CAP = 1u << 20;

__device__ __forceinline__ float bflo(unsigned v) { return __builtin_bit_cast(float, v << 16); }
__device__ __forceinline__ float bfhi(unsigned v) { return __builtin_bit_cast(float, v & 0xffff0000u); }
__device__ __forceinline__ unsigned f2bf(float f) { return __builtin_bit_cast(unsigned short, (__bf16)f); }
__device__ __forceinline__ float rbf(float f) { return bflo(f2bf(f)); }
__device__ __forceinline__ unsigned pk2(float lo, float hi) {
  bf16x2 v = {(__bf16)lo, (__bf16)hi};
  return __builtin_bit_cast(unsigned, v);
}
__device__ __forceinline__ float dot2(unsigned k, unsigned q, float acc) {
  return __builtin_amdgcn_fdot2_f32_bf16(__builtin_bit_cast(bf16x2, k), __builtin_bit_cast(bf16x2, q), acc, false);
}
template <int CTRL> __device__ __forceinline__ unsigned dppu(unsigned x) { return __builtin_amdgcn_mov_dpp((int)x, CTRL, 0xf, 0xf, true); }
template <int CTRL> __device__ __forceinline__ float dpp(float x) { return __uint_as_float(dppu<CTRL>(__float_as_uint(x))); }
constexpr int XOR1 = 0xB1, XOR2 = 0x4E, HMIR = 0x141, MIR = 0x140, ROR4 = 0x124, ROR8 = 0x128, SHL = 0x100;
__device__ __forceinline__ float row16_sum(float x) {
  x += dpp<XOR1>(x); x += dpp<XOR2>(x); x += dpp<HMIR>(x); x += dpp<MIR>(x);
  return x;
}
__device__ __forceinline__ float tr32(float a, float b) {
  auto s = __builtin_amdgcn_permlane32_swap(__float_as_uint(a), __float_as_uint(b), false, false);
  return __uint_as_float(s[0]) + __uint_as_float(s[1]);
}
__device__ __forceinline__ float tr16(float a, float b) {
  auto s = __builtin_amdgcn_permlane16_swap(__float_as_uint(a), __float_as_uint(b), false, false);
  return __uint_as_float(s[0]) + __uint_as_float(s[1]);
}
__device__ __forceinline__ float x16_sum(float x) { return tr16(x, x); }
__device__ __forceinline__ float x32_sum(float x) { return tr32(x, x); }
__device__ __forceinline__ float x16_max(float x) {
  auto s = __builtin_amdgcn_permlane16_swap(__float_as_uint(x), __float_as_uint(x), false, false);
  return fmaxf(__uint_as_float(s[0]), __uint_as_float(s[1]));
}
__device__ __forceinline__ float x32_max(float x) {
  auto s = __builtin_amdgcn_permlane32_swap(__float_as_uint(x), __float_as_uint(x), false, false);
  return fmaxf(__uint_as_float(s[0]), __uint_as_float(s[1]));
}
__device__ __forceinline__ float wave_sum(float x) { return x32_sum(x16_sum(row16_sum(x))); }
__device__ __forceinline__ unsigned canonu(unsigned u) { return min(u, SENT - 1u); }
__device__ __forceinline__ unsigned canon(float f) { return canonu(__float_as_uint(f)); }
__device__ __forceinline__ f32x4 mfma(u32x4 a, u32x4 b, f32x4 c) {
  return __builtin_amdgcn_mfma_f32_16x16x32_bf16(__builtin_bit_cast(bf16x8, a), __builtin_bit_cast(bf16x8, b), c, 0, 0, 0);
}
template <int N> __device__ __forceinline__ void waitv() { asm volatile("s_waitcnt vmcnt(%0)" ::"i"(N) : "memory"); }
__device__ __forceinline__ void waitl() { asm volatile("s_waitcnt lgkmcnt(0)" ::: "memory"); }
__device__ __forceinline__ void bar() { waitl(); __builtin_amdgcn_s_barrier(); asm volatile("" ::: "memory"); }
__device__ __forceinline__ u64 mix64(u64 z) {
  z += 0x9E3779B97F4A7C15ull; z = (z ^ (z >> 30)) * 0xBF58476D1CE4E5B9ull; z = (z ^ (z >> 27)) * 0x94D049BB133111EBull;
  return z ^ (z >> 31);
}
#define STR2(x) #x
#define STR(x) STR2(x)
#define NTS " nt"
#define BIGINC "0xe800"
#define BIGHI ""
#define DMA4(a, b, c, d) \
  "global_load_lds_dwordx4 %[" #a "], s[60:61] offset:0" NTS "\n\t" \
  "global_load_lds_dwordx4 %[" #b "], s[60:61] offset:1024" NTS "\n\t" \
  "global_load_lds_dwordx4 %[" #c "], s[60:61] offset:2048" NTS "\n\t" \
  "global_load_lds_dwordx4 %[" #d "], s[60:61] offset:3072" NTS "\n\t"
__device__ __forceinline__ void stream_steps(const unsigned (&v)[16], const char*& p, int& t, int tend, int& w, int& pub, int& sch,
                                             unsigned a_free, unsigned a_full) {
  unsigned vf, vt, ve;
  asm volatile(
      "s_mov_b64 s[60:61], %[p]\n\t"
      "s_getpc_b64 s[62:63]\n"
      "0:\n\t"
      "s_add_u32 s62, s62, 9f-0b-8*" STR(WMIN) "\n\t"
      "s_addc_u32 s63, s63, 0\n\t"
      "s_mov_b32 s73, 0x100000\n"
      "1:\n\t"
      "s_memrealtime s[64:65]\n\t"
      "ds_read_b32 %[vf], %[af]\n\t"
      "s_lshl_b32 s66, %[w], 3\n\t"
      "s_add_u32 s70, s62, s66\n\t"
      "s_addc_u32 s71, s63, 0\n\t"
      "s_lshl_b32 s66, %[t], 14\n\t"
      "s_and_b32 m0, s66, 0x1c000\n\t"
      "s_swappc_b64 s[68:69], s[70:71]\n\t"
      DMA4(v0, v1, v2, v3)
      "s_add_u32 m0, m0, 0x1000\n\t"
      "s_swappc_b64 s[68:69], s[70:71]\n\t"
      DMA4(v4, v5, v6, v7)
      "s_add_u32 m0, m0, 0x1000\n\t"
      "s_swappc_b64 s[68:69], s[70:71]\n\t"
      DMA4(v8, v9, v10, v11)
      "s_add_u32 m0, m0, 0x1000\n\t"
      "s_swappc_b64 s[68:69], s[70:71]\n\t"
      DMA4(v12, v13, v14, v15)
      "s_lshl_b32 s66, %[t], 4\n\t"
      "s_add_i32 s66, s66, 12\n\t"
      "s_sub_i32 s66, s66, %[w]\n\t"
      "s_ashr_i32 s66, s66, 4\n\t"
      "s_max_i32 %[pub], %[pub], s66\n\t"
      "v_mov_b32 %[vt], %[pub]\n\t"
      "s_waitcnt lgkmcnt(0)\n\t"
      "v_readlane_b32 s65, %[vf], 63\n\t"
      "s_lshl_b32 s64, s64, 7\n\t"
      "s_sub_i32 s64, s64, %[sch]\n\t"
      "v_mov_b32 %[ve], s64\n\t"
      "s_add_i32 %[sch], %[sch], " STR(TSCHED) "\n\t"
      "ds_write2_b32 %[al], %[vt], %[ve] offset1:64\n\t"
      "s_sub_i32 s64, s64, s65\n\t"
      "s_ashr_i32 s64, s64, 4\n\t"
      "s_mulk_i32 s64, " STR(GAIN) "\n\t"
      "s_ashr_i32 s64, s64, 11\n\t"
      "s_add_i32 s64, s64, " STR(W0) "\n\t"
      "s_max_i32 s64, s64, " STR(WMIN) "\n\t"
      "s_min_i32 %[w], s64, " STR(WMAX) "\n\t"
      "s_and_b32 s66, %[t], 3\n\t"
      "s_movk_i32 s67, 0x800\n\t"
      "s_mov_b32 s72, 0\n\t"
      "s_cmp_eq_u32 s66, 3\n\t"
      "s_cmov_b32 s67, " BIGINC "\n\t"
      BIGHI
      "s_add_u32 s60, s60, s67\n\t"
      "s_addc_u32 s61, s61, s72\n\t"
      "s_add_i32 %[t], %[t], 1\n\t"
      "s_add_i32 s66, %[t], -7\n"
      "2:\n\t"
      "v_cmp_gt_i32 s[64:65], s66, %[vf]\n\t"
      "s_cmp_lg_u32 s64, 0\n\t"
      "s_cbranch_scc1 3f\n"
      "4:\n\t"
      "s_cmp_lg_u32 %[t], %[tend]\n\t"
      "s_cbranch_scc1 1b\n\t"
      "s_branch 8f\n"
      "3:\n\t"
      "s_cmp_eq_u32 s73, 0\n\t"
      "s_cbranch_scc1 4b\n\t"
      "s_sub_u32 s73, s73, 1\n\t"
      "ds_read_b32 %[vf], %[af]\n\t"
      "s_waitcnt lgkmcnt(0)\n\t"
      "s_branch 2b\n"
      "9:\n\t"
      ".set i_, " STR(WMIN) "\n\t.rept " STR(WMAX) "-" STR(WMIN) "+1\n\ts_waitcnt vmcnt(i_)\n\ts_setpc_b64 s[68:69]\n\t.set i_, i_+1\n\t.endr\n"
      "8:\n\t"
      "s_mov_b64 %[p], s[60:61]"
      : [p] "+s"(p), [t] "+s"(t), [w] "+s"(w), [pub] "+s"(pub), [sch] "+s"(sch), [vf] "=&v"(vf), [vt] "=&v"(vt), [ve] "=&v"(ve)
      : [tend] "s"(tend), [af] "v"(a_free), [al] "v"(a_full),
        [v0] "v"(v[0]), [v1] "v"(v[1]), [v2] "v"(v[2]), [v3] "v"(v[3]), [v4] "v"(v[4]), [v5] "v"(v[5]), [v6] "v"(v[6]), [v7] "v"(v[7]),
        [v8] "v"(v[8]), [v9] "v"(v[9]), [v10] "v"(v[10]), [v11] "v"(v[11]), [v12] "v"(v[12]), [v13] "v"(v[13]), [v14] "v"(v[14]), [v15] "v"(v[15])
      : "memory", "scc", "s60", "s61", "s62", "s63", "s64", "s65", "s66", "s67", "s68", "s69", "s70", "s71", "s72", "s73");
}
static_assert(WMAX <= 44 && WMIN >= 13 && WMIN <= W0 && W0 <= WMAX && WMIN <= WSTART && WSTART <= WMAX, "window");
template <int SLEEP> __device__ __forceinline__ unsigned lds_wait(LAS unsigned char* L, int off, unsigned want) {
  unsigned v = 0;
#pragma unroll 1
  for (unsigned s = 0; s < SPIN_CAP; ++s) {
    v = __builtin_amdgcn_readfirstlane(*(volatile LAS unsigned*)(L + off));
    if (v >= want) break;
    if (SLEEP) __builtin_amdgcn_s_sleep(SLEEP);
  }
  asm volatile("" ::: "memory");
  return v;
}
__device__ __forceinline__ int ccol(int n) { return 8 * (n & 7) + 4 * (n >> 3); }
template <int LD, int RS, int RK> __device__ __forceinline__ int tile_voff(int lane) { return ((RS * ((lane >> 3) & 1) + RK * (lane >> 4)) * LD + 8 * (lane & 7)) * 2; }
template <int LD> __device__ __forceinline__ void ldtile(u32x4 (&raw)[8], __amdgpu_buffer_rsrc_t r, int elem0, int voff) {
#pragma unroll
  for (int j = 0; j < 8; ++j) raw[j] = __builtin_amdgcn_raw_buffer_load_b128(r, voff, (elem0 + j * LD) * 2, AUX_NT);
}
__device__ __forceinline__ void pairrows(const u32x4 (&raw)[8], u32x4 (&f)[8]) {
#pragma unroll
  for (int jp = 0; jp < 4; ++jp)
#pragma unroll
    for (int d = 0; d < 4; ++d) {
      f[2 * d][jp] = __builtin_amdgcn_perm(raw[2 * jp + 1][d], raw[2 * jp][d], 0x05040100u);
      f[2 * d + 1][jp] = __builtin_amdgcn_perm(raw[2 * jp + 1][d], raw[2 * jp][d], 0x07060302u);
    }
}
__device__ __forceinline__ void pin8(u32x4 (&f)[8]) {
  asm volatile("" : "+v"(f[0]), "+v"(f[1]), "+v"(f[2]), "+v"(f[3]), "+v"(f[4]), "+v"(f[5]), "+v"(f[6]), "+v"(f[7]));
}
__device__ __forceinline__ void mkfrag(const u32x4 (&raw)[8], u32x4 (&f)[8]) {
  u32x4 p[8];
  pairrows(raw, p);
#pragma unroll
  for (int c = 0; c < 4; ++c)
#pragma unroll
    for (int jp = 0; jp < 4; ++jp) {
      f[c][jp] = __builtin_amdgcn_update_dpp((int)p[c][jp], (int)p[4 + c][jp], ROR8, 0xf, 0xc, false);
      f[4 + c][jp] = __builtin_amdgcn_update_dpp((int)p[4 + c][jp], (int)p[c][jp], ROR8, 0xf, 0x3, false);
    }
  pin8(f);
}
__device__ __forceinline__ void mld4(u32x4& v, u64 m, unsigned voff, const unsigned char* sb) {
  asm volatile("s_mov_b64 exec, %1\n\tglobal_load_dwordx4 %0, %2, %3 sc1\n\ts_mov_b64 exec, -1" : "+v"(v) : "s"(m), "v"(voff), "s"(sb) : "memory");
}
__device__ __forceinline__ void mld2(u32x2& v, u64 m, unsigned voff, const unsigned char* sb) {
  asm volatile("s_mov_b64 exec, %1\n\tglobal_load_dwordx2 %0, %2, %3 sc1\n\ts_mov_b64 exec, -1" : "+v"(v) : "s"(m), "v"(voff), "s"(sb) : "memory");
}
__device__ __forceinline__ u64 inval4(u32x4 v) { return __ballot(max(max(v[0], v[1]), max(v[2], v[3])) == SENT); }
__device__ __forceinline__ u64 inval2(u32x2 v) { return __ballot(max(v[0], v[1]) == SENT); }

#define ST(i) do {} while (0)
#define STY(i) ST(i)

template <typename T> __device__ __forceinline__ void pin(T& v) { asm volatile("" : "+v"(v)); }
typedef short s16x4 __attribute__((ext_vector_type(4)));
__device__ __forceinline__ u32x2 tr_read(LAS unsigned char* p) {
  return __builtin_bit_cast(u32x2, __builtin_amdgcn_ds_read_tr16_b64_v4i16((LAS s16x4*)p));
}
template <int R, int SLP> __device__ __forceinline__ void gather4(u32x4 (&v)[4], unsigned off, const unsigned char* base, int stride) {
  u64 mk[4];
#pragma unroll
  for (int i = 0; i < 4; ++i) mk[i] = inval4(v[i]);
#pragma unroll 1
  for (unsigned spins = 0; spins < SPIN_CAP; ++spins) {
#pragma unroll
    for (int i = 0; i < 4; ++i) mld4(v[i], mk[i], off, base + i * stride);
    if (SLP) __builtin_amdgcn_s_sleep(SLP);
    asm volatile("s_waitcnt vmcnt(%4)" : "+v"(v[0]), "+v"(v[1]), "+v"(v[2]), "+v"(v[3]) : "i"(4 * (R - 1)) : "memory");
#pragma unroll
    for (int i = 0; i < 4; ++i) mk[i] = inval4(v[i]);
    if (((mk[0] | mk[1]) | (mk[2] | mk[3])) == 0) break;
  }
}
template <int R> __device__ __forceinline__ void drain4(u32x4 (&v)[4]) {
  if (R > 1) asm volatile("s_waitcnt vmcnt(0)" : "+v"(v[0]), "+v"(v[1]), "+v"(v[2]), "+v"(v[3])::"memory");
}
__device__ __forceinline__ void warm_code(int lane) {
  u64 pc;
  asm volatile("s_getpc_b64 %0" : "=s"(pc));
  const unsigned t = *(const GAS unsigned*)((pc & ~63ull) + lane * 64);
  asm volatile("" ::"v"(t));
}
#define LP(T, a) ((LAS T*)(size_t)(a))
__device__ __forceinline__ int role_b(int g) {
  const int xc = g & 7, i = g >> 3;
  if (CXCD == 1) return xc < 7 && i < 7 ? xc * 8 + i : (xc == 0 && i < 14 ? 56 + i - 7 : -1);
  if (CXCD == 3) return xc < 6 && i < 7 ? xc * 8 + i : (xc < 2 && i < 14 ? (6 + xc) * 8 + i - 7 : -1);
  if (CXCD == 5) return xc < 4 && i < 14 ? (i < 7 ? xc * 8 + i : (4 + xc) * 8 + i - 7) : -1;
  return i < 7 ? xc * 8 + i : -1;
}
__device__ __forceinline__ int role_c(int g) {
  const int xc = g & 7, i = g >> 3;
  if (CXCD == 1) return xc == 7 && i < 28 ? i : -1;
  if (CXCD == 3) return xc >= 6 && i < 14 ? (xc - 6) * 14 + i : -1;
  if (CXCD == 5) return xc >= 4 && i < 7 ? (xc - 4) * 7 + i : -1;
  if (CXCD == 7) return xc < 7 && i >= 7 && i < 11 ? 4 * xc + i - 7 : -1;
  if (CXCD == 8) return i >= 7 && (i - 7) * 8 + xc < 28 ? (i - 7) * 8 + xc : -1;
  if (CXCD == 2) return xc >= 6 && i >= 7 && i < 21 ? (xc - 6) * 14 + i - 7 : -1;
  return xc >= 4 && i >= 7 && i < 14 ? (xc - 4) * 7 + i - 7 : -1;
}

__global__ void __launch_bounds__(NTHR, 3)
fused_block(const bf16_t* __restrict__ x, const bf16_t* __restrict__ kc, const bf16_t* __restrict__ vc,
            const bf16_t* __restrict__ wq, const bf16_t* __restrict__ wo, const bf16_t* __restrict__ wg,
            const bf16_t* __restrict__ wu, const bf16_t* __restrict__ wd, bf16_t* __restrict__ out, unsigned char* ws) {
  extern __shared__ __attribute__((aligned(16))) unsigned char smem[];
  LAS unsigned char* L = (LAS unsigned char*)smem;
  const int tid = threadIdx.x;
  if (FASTPRO && MAPX == 0) {
    unsigned vv;
    asm volatile(
        "s_load_dwordx2 s[60:61], %[ka], 0x8\n\t"
        "v_readfirstlane_b32 s62, %[tid]\n\t"
        "v_lshlrev_b32 %[vv], 4, %[tid]\n\t"
        "s_lshr_b32 s63, %[g], 6\n\t"
        "s_cmp_eq_u32 s63, 2\n\t"
        "s_cselect_b32 s62, 64, s62\n\t"
        "s_cmp_gt_u32 s62, 63\n\t"
        "s_cbranch_scc1 2f\n\t"
        "s_lshr_b32 s63, %[g], 3\n\t"
        "s_add_u32 s63, s63, %[g]\n\t"
        "s_and_b32 s63, s63, 7\n\t"
        "s_andn2_b32 s62, %[g], 7\n\t"
        "s_or_b32 s62, s62, s63\n\t"
        "s_lshl_b32 s62, s62, 20\n\t"
        "s_mov_b32 m0, 0\n\t"
        "s_movk_i32 s63, 0x2000\n\t"
        "s_waitcnt lgkmcnt(0)\n\t"
        "s_add_u32 s60, s60, s62\n\t"
        "s_addc_u32 s61, s61, 0\n"
        "1:\n\t"
        "global_load_lds_dwordx4 %[vv], s[60:61]" NTS "\n\t"
        "s_addk_i32 m0, 0x400\n\t"
        "s_and_b32 s62, m0, 0x1c00\n\t"
        "s_cselect_b32 s62, s63, 0xffff2400\n\t"
        "v_add_u32 %[vv], s62, %[vv]\n\t"
        "s_bitcmp0_b32 m0, 15\n\t"
        "s_cbranch_scc1 1b\n"
        "2:\n\t"
        "s_waitcnt lgkmcnt(0)"
        : [vv] "=&v"(vv)
        : [ka] "s"(__builtin_amdgcn_kernarg_segment_ptr()), [tid] "v"(tid), [g] "s"(blockIdx.x)
        : "memory", "scc", "s60", "s61", "s62", "s63");
  }
  const int lane = tid & 63, wave = __builtin_amdgcn_readfirstlane(tid >> 6);
  const int g = blockIdx.x;
  const int gi = g >> 3;
  const int gl = MAPX == 0 ? (g & ~7) | ((g + gi) & 7) : MAPX == 1 ? (g & 7) * 32 + gi : ((gi >> 1) * 8 + ((g + (gi >> 1)) & 7)) * 2 + (gi & 1);
  const int bh = gl >> 3;

  const size_t chunk = (size_t)gl << 20;
  if (CODEPF && wave != 0) {
    u64 pc;
    const unsigned co = min((unsigned)(tid - 64) * 64u, (unsigned)CODEPF - 64u);
    asm volatile("s_getpc_b64 %0\n\ts_mov_b32 m0, %2\n\ts_nop 0\n\tglobal_load_lds_dword %1, %0" : "=&s"(pc) : "v"(co), "s"(L_RED) : "memory");
  }
  int wave0 = wave;
  asm volatile("" : "+s"(wave0));
  if (wave0 == 0) {
    __builtin_amdgcn_s_setprio(LPRIO);
    unsigned vo[16];
#pragma unroll
    for (int i = 0; i < 16; ++i) vo[i] = lane * 16 + (i & 7) * 8192 + (i >> 3) * 1024 - (i & 3) * 1024;
    unsigned a_free = lane < 32 ? L_FREE + (lane & 3) * 4 : L_CORR, a_full = L_FULL + lane * 4;
#pragma unroll
    for (int i = 0; i < 16; ++i) pin(vo[i]);
    pin(a_free); pin(a_full);
    __builtin_amdgcn_sched_barrier(0);
    const bool qp = (gi >> 3) == 2;
    const int ts = FASTPRO && MAPX == 0 && !qp ? 2 : 0;
    const char* p = (const char*)kc + chunk + ts * 2048;
    int t = ts, pub = 0, w = qp ? WMIN : WSTART, sch = ts * TSCHED;
    {
      const unsigned e0 = ((unsigned)__builtin_amdgcn_s_memrealtime() + (qp ? 1000 : -PAST)) << 7;
      *LP(unsigned, L_CTL + lane * 4) = lane == (L_CORR - L_CTL) / 4 ? e0 : 0u;
    }
    bar();
#pragma unroll 1
    for (int tend = NPH; tend <= NSTEP; tend += NPH) {
      stream_steps(vo, p, t, tend, w, pub, sch, a_free, a_full);
      p = (const char*)vc + chunk;
#pragma unroll
      for (int i = 0; i < 16; ++i) vo[i] ^= (lane >> 5) << 7;
    }
    volatile LAS int* vfull = (volatile LAS int*)(L + L_FULL) + lane;
    waitv<32>(); *vfull = NSTEP - 2;
    waitv<16>(); *vfull = NSTEP - 1;
    waitv<0>();  *vfull = NSTEP;
    ST(3);
    asm volatile("s_endpgm" ::: "memory");
  }
  if (wave == 1 && lane < 32) { ((LAS unsigned*)(L + L_ZERO))[lane] = 0u; ((LAS unsigned*)(L + L_ST))[lane] = 0u; }
  if (wave <= 4 || !QFIRST) bar();
  const __amdgpu_buffer_rsrc_t rs = __builtin_amdgcn_make_buffer_rsrc(ws, 0, W_END, 0x00020000);
  const int n = lane & 15, kg = lane >> 4;
  if (wave <= 4) {
    const int ci = wave - 1;
    unsigned full = 0;
    volatile LAS unsigned* vfree = (volatile LAS unsigned*)(L + L_FREE) + ci;
    {
      const int lo = (8 * ci + kg) * 512 + 16 * n;
      auto fetch = [&](int t, u32x4 (&d)[4]) {
        if ((int)full <= t) full = lds_wait<CSLEEP>(L, L_FULL, t + 1);
        LAS unsigned char* sb = L + (t & (NSLOT - 1)) * SLOT_BYTES + lo;
        d[0] = *(LAS u32x4*)(sb); d[1] = *(LAS u32x4*)(sb + 256); d[2] = *(LAS u32x4*)(sb + 2048); d[3] = *(LAS u32x4*)(sb + 2304);
        waitl();
        *vfree = t + 1;
        asm volatile("" : "+v"(d[0]), "+v"(d[1]), "+v"(d[2]), "+v"(d[3])::"memory");
      };
      u32x4 pre[PRE][4];
#pragma unroll
      for (int t = 0; t < PRE; ++t) fetch(t, pre[t]);
      lds_wait<1>(L, L_QRDY, 1u);
      const u32x4 q0 = *(LAS u32x4*)(L + L_Q + 16 * n), q1 = *(LAS u32x4*)(L + L_Q + 256 + 16 * n);
      constexpr float C2 = 0.0625f * 1.4426950408889634f;
      float m = -INFINITY;
      LAS float* sp = (LAS float*)(L + L_P) + 8 * ci + kg + 4 * (n & 1);
      auto score = [&](int t, const u32x4 (&k)[4]) {
        float s0 = 0.f, s1 = 0.f;
#pragma unroll
        for (int j = 0; j < 4; ++j) { s0 = dot2(k[0][j], q0[j], s0); s1 = dot2(k[2][j], q0[j], s1); }
#pragma unroll
        for (int j = 0; j < 4; ++j) { s0 = dot2(k[1][j], q1[j], s0); s1 = dot2(k[3][j], q1[j], s1); }
        s0 += dpp<XOR1>(s0); s1 += dpp<XOR1>(s1);
        float s = (n & 1) ? s1 : s0;
        s += dpp<XOR2>(s); s += dpp<ROR4>(s); s += dpp<ROR8>(s);
        s = rbf(s) * C2;
        m = fmaxf(m, s);
        if (n < 2) sp[t * 32] = s;
      };
#pragma unroll
      for (int t = 0; t < PRE; ++t) score(t, pre[t]);
#pragma unroll 1
      for (int t = PRE; t < NPH; ++t) { u32x4 k[4]; fetch(t, k); score(t, k); }
      m = fmaxf(m, dpp<XOR1>(m));
      m = x32_max(x16_max(m));
      if (lane == 0) ((LAS float*)(L + L_MW))[ci] = m;
      waitl();
      if (lane == 0) __hip_atomic_fetch_add((LAS unsigned*)(L + L_C1), 1u, RLX_WG);
      lds_wait<0>(L, L_C1, 4u);
      const f32x4 mw = *(LAS f32x4*)(L + L_MW);
      const float M = fmaxf(fmaxf(mw[0], mw[1]), fmaxf(mw[2], mw[3]));
      LAS unsigned char* pp = L + L_P + (ci * 64 + lane) * 32;
      const f32x4 e0 = *(LAS f32x4*)pp, e1 = *(LAS f32x4*)(pp + 16);
      u32x4 hi, lo2;
      float l = 0.f;
#pragma unroll
      for (int j = 0; j < 4; ++j) {
        const float a = __builtin_amdgcn_exp2f((j < 2 ? e0[2 * j] : e1[2 * j - 4]) - M), b = __builtin_amdgcn_exp2f((j < 2 ? e0[2 * j + 1] : e1[2 * j - 3]) - M);
        l += a + b;
        hi[j] = pk2(a, b);
        lo2[j] = pk2(a - bflo(hi[j]), b - bfhi(hi[j]));
      }
      *(LAS u32x4*)pp = hi; *(LAS u32x4*)(pp + 16) = lo2;
      l = wave_sum(l);
      if (lane == 0) ((LAS float*)(L + L_LW))[ci] = l;
      waitl();
      if (lane == 0) __hip_atomic_fetch_add((LAS unsigned*)(L + L_C2), 1u, RLX_WG);
      lds_wait<0>(L, L_C2, 4u);
    }
    {
      const int q = n >> 2;
      const int lo = q * 512 + (ci ^ (q & 1)) * 128 + kg * 32 + (n & 3) * 8;
      const bool act = q == kg && (n & 3) < 2;
      unsigned pa = act ? L_P + (n & 1) * 16 : L_ZERO;
      const unsigned pinc = act ? 128u : 0u;
      int off = (lane & 1) ? OOB : W_RECA + gl * RECA + (64 * ci + lane) * 2;
      pin(off);
      f32x4 acc = {0.f, 0.f, 0.f, 0.f};
      float sc = 0.f;
#pragma unroll 1
      for (int t = NPH; t < NSTEP; ++t) {
        u32x4 a[4];
#pragma unroll
        for (int m = 0; m < 4; ++m) a[m] = *(LAS u32x4*)(L + pa + 32 * m);
        pa += pinc;
        if (t == NSTEP - 4) { lds_wait<1>(L, L_SCRDY, 1u); sc = *(LAS float*)(L + L_SCALE); }
        if ((int)full <= t) full = lds_wait<VSLEEP>(L, L_FULL, t + 1);
        LAS unsigned char* sb = L + (t & (NSLOT - 1)) * SLOT_BYTES + lo;
        u32x2 v[8];
#pragma unroll
        for (int j = 0; j < 8; ++j) v[j] = tr_read(sb + 2048 * j);
#pragma unroll
        for (int m = 0; m < 4; ++m) acc = mfma(a[m], u32x4{v[2 * m][0], v[2 * m][1], v[2 * m + 1][0], v[2 * m + 1][1]}, acc);
        waitl();
        *vfree = t + 1;
      }
      if (wave == 1) ST(4);
      const float o = (acc[0] + acc[1]) * sc;
      __builtin_amdgcn_raw_buffer_store_b32(canonu(pk2(o, dpp<XOR1>(o))), rs, off, 0, AUX_SC1);
      if (lane == 0) __hip_atomic_fetch_add((LAS unsigned*)(L + L_PUB), 1u, RLX_WG);
      if (wave == 1) ST(5);
    }
    return;
  }
  const int tw = wave - 5;
  const int rb = role_b(g), jc = role_c(g);
  const int ks = rb >> 3, cs = rb & 7;
  auto invalidate = [&]() {
    const u32x4 sv = {SENT, SENT, SENT, SENT};
    __builtin_amdgcn_raw_buffer_store_b128(sv, rs, lane < 32 ? W_RECA + gl * RECA + 16 * lane : OOB, 0, AUX_SC1);
    if (rb >= 0) __builtin_amdgcn_raw_buffer_store_b128(sv, rs, lane < 32 ? W_RECB + ((ks * 2 + kg) * D + 64 * cs + 4 * n) * 4 : OOB, 0, AUX_SC1);
    if (jc >= 0) {
#pragma unroll
      for (int i = 0; i < 4; ++i) __builtin_amdgcn_raw_buffer_store_b128(sv, rs, lane < 56 ? W_RECC + jc * (2 * D * 4) + (56 * i + lane) * 16 : OOB, 0, AUX_SC1);
    }
  };
  if (EARLYINV && tw == 4) invalidate();
  if (EARLYINV == 2 && tw == 0 && (gi >> 3) == 2)
    __builtin_amdgcn_raw_buffer_store_b128(u32x4{0u, 0u, 0u, 0u}, rs, W_QG + ((lane >> 5) * HD + 64 * ((g & 7) * 8 + (gi & 7))) * 8 + (lane & 31) * 16, 0, AUX_SC1);
  u64 tag;
  {
    const bool qprod = (gi >> 3) == 2;
    const int qc = 64 * ((g & 7) * 8 + (gi & 7));
    unsigned z = 0;
    pin(z);
    const u64 gen = __hip_atomic_load((GAS u64*)(ws + W_GEN + z), RLX_AGENT);
    if (qprod) {
      u32x4 wqr[8], xa[2];
      const int row0 = 64 * tw + 8 * (lane >> 3);
      u32x4 xr[2];
#pragma unroll
      for (int b = 0; b < 2; ++b) { xa[b] = *(const GAS u32x4*)(x + b * D + 8 * min(lane, 55)); xr[b] = *(const GAS u32x4*)(x + b * D + row0); }
#pragma unroll
      for (int i = 0; i < 8; ++i) wqr[i] = __builtin_nontemporal_load((const GAS u32x4*)(wq + (size_t)(row0 + i) * HD + qc + 8 * (lane & 7)));
      __builtin_amdgcn_sched_barrier(0);
      if (QFIRST) { __builtin_amdgcn_s_barrier(); asm volatile("" ::: "memory"); }
      __builtin_amdgcn_sched_barrier(0);
      float a[2][8];
#pragma unroll
      for (int b = 0; b < 2; ++b) {
        if (lane >= 56) xa[b] = u32x4{0u, 0u, 0u, 0u};
        float xs[8]; float s = 0.f;
#pragma unroll
        for (int i = 0; i < 4; ++i) { xs[2 * i] = bflo(xa[b][i]); xs[2 * i + 1] = bfhi(xa[b][i]); s += xs[2 * i] + xs[2 * i + 1]; }
        const float mean = wave_sum(s) * (1.f / D);
        float qv = 0.f;
#pragma unroll
        for (int i = 0; i < 8; ++i) { const float d = xs[i] - mean; qv = fmaf(d, d, qv); }
        if (lane >= 56) qv = 0.f;
        const float var = wave_sum(qv) * (1.f / D);
        const float mb = rbf(mean);
        const float rsd = __builtin_amdgcn_rcpf(rbf(__builtin_amdgcn_sqrtf(rbf(rbf(var) + 1e-5f))));
#pragma unroll
        for (int i = 0; i < 4; ++i) {
          const unsigned d2 = pk2(bflo(xr[b][i]) - mb, bfhi(xr[b][i]) - mb);
          xr[b][i] = pk2(bflo(d2) * rsd, bfhi(d2) * rsd);
        }
#pragma unroll
        for (int j = 0; j < 8; ++j) a[b][j] = 0.f;
      }
      u32x4 pr[8];
      pairrows(wqr, pr);
#pragma unroll
      for (int b = 0; b < 2; ++b)
#pragma unroll
        for (int j = 0; j < 8; ++j)
#pragma unroll
          for (int i = 0; i < 4; ++i) a[b][j] = dot2(pr[j][i], xr[b][i], a[b][j]);
      float b8[8], b4[4];
#pragma unroll
      for (int j = 0; j < 8; ++j) b8[j] = tr32(a[0][j], a[1][j]);
#pragma unroll
      for (int j = 0; j < 4; ++j) { b4[j] = tr16(b8[j], b8[j + 4]); b4[j] += dpp<ROR8>(b4[j]); }
      if ((lane & 8) == 0) *LP(f32x4, L_QPART + (tw * 128 + (lane >> 5) * 64 + 8 * (lane & 7) + 4 * ((lane >> 4) & 1)) * 4) = f32x4{b4[0], b4[1], b4[2], b4[3]};
      waitl();
      if (lane == 0) __hip_atomic_fetch_add((LAS unsigned*)(L + L_QCNT), 1u, RLX_WG);
    } else if (QFIRST) {
      __builtin_amdgcn_s_barrier(); asm volatile("" ::: "memory");
    }
    tag = mix64(((u64)__builtin_amdgcn_readfirstlane((unsigned)(gen >> 32)) << 32) | __builtin_amdgcn_readfirstlane((unsigned)gen));
    if (tw == 0) {
      const u64 t48 = tag & ~0xFFFFull;
      if (qprod) {
        lds_wait<0>(L, L_QCNT, 7u);
        ST(1);
#pragma unroll
        for (int b = 0; b < 2; ++b) {
          float qs = 0.f;
#pragma unroll
          for (int w = 0; w < 7; ++w) qs += *LP(float, L_QPART + (w * 128 + b * 64 + lane) * 4);
          __hip_atomic_store((GAS u64*)(ws + W_QG) + b * HD + qc + lane, t48 | f2bf(qs), RLX_AGENT);
        }
      }
      lds_wait<2>(L, L_FULL, qprod ? 1 : QPOLL);
      GAS u64* qg = (GAS u64*)(ws + W_QG) + bh * DH + lane;
      u64 v[4];
#pragma unroll 1
      for (unsigned spins = 0; spins < SPIN_CAP; ++spins) {
        bool ok = true;
#pragma unroll
        for (int i = 0; i < 4; ++i) v[i] = __hip_atomic_load(qg + 64 * i, RLX_AGENT);
#pragma unroll
        for (int i = 0; i < 4; ++i) ok &= (v[i] & ~0xFFFFull) == t48;
        if (__all(ok)) break;
        if (QSLEEP) __builtin_amdgcn_s_sleep(QSLEEP);
      }
#pragma unroll
      for (int i = 0; i < 4; ++i) ((LAS unsigned short*)(L + L_Q))[64 * i + lane] = (unsigned short)v[i];
      waitl();
      *(volatile LAS unsigned*)(L + L_QRDY) = 1u;
      ST(2);
    }
  }
  auto exchange = [&]() {
    if (!EARLYINV && INVAT) { lds_wait<16>(L, L_FULL, INVAT + gi); invalidate(); }
    lds_wait<8>(L, L_C2, 4u);
    if (XSTAG) lds_wait<16>(L, L_FULL, NPH + 2 + gi * XSTAG / 32);
    if (!EARLYINV && !INVAT) invalidate();
    const f32x4 mw = *(LAS f32x4*)(L + L_MW), lw = *(LAS f32x4*)(L + L_LW);
    const float Mw = fmaxf(fmaxf(mw[0], mw[1]), fmaxf(mw[2], mw[3])), Lw = (lw[0] + lw[1]) + (lw[2] + lw[3]);
    __builtin_amdgcn_raw_buffer_store_b64(u32x2{__float_as_uint(Mw), __float_as_uint(Lw)}, rs, lane == 0 ? W_ML + gl * 8 : OOB, 0, AUX_SC1);
    waitv<0>();
    if (lane == 0) __hip_atomic_store((GAS u64*)(ws + W_FLAG) + gl, tag, RLX_AGENT);
    lds_wait<16>(L, L_FULL, ML_STEP + gi * RSTAG / 32);
    const bool all = rb >= 0 || jc >= 0;
    GAS u64* fp = (GAS u64*)(ws + W_FLAG) + (all ? lane : 8 * bh + (lane & 7));
    const int fs = all ? 64 : 0;
#pragma unroll 1
    for (unsigned spins = 0; spins < SPIN_CAP; ++spins) {
      bool ok = true;
#pragma unroll
      for (int i = 0; i < 4; ++i) ok &= __hip_atomic_load(fp + fs * i, RLX_AGENT) == tag;
      if (__all(ok)) break;
      __builtin_amdgcn_s_sleep(16);
    }
    asm volatile("" ::: "memory");
    const u64 ml = __hip_atomic_load((GAS u64*)(ws + W_ML) + 8 * bh + (lane & 7), RLX_AGENT);
    const float mi = __uint_as_float((unsigned)ml), li = __uint_as_float((unsigned)(ml >> 32));
    float Mg = fmaxf(mi, dpp<XOR1>(mi)); Mg = fmaxf(Mg, dpp<XOR2>(Mg)); Mg = fmaxf(Mg, dpp<HMIR>(Mg));
    float Lg = li * __builtin_amdgcn_exp2f(mi - Mg);
    Lg += dpp<XOR1>(Lg); Lg += dpp<XOR2>(Lg); Lg += dpp<HMIR>(Lg);
    *(volatile LAS float*)(L + L_SCALE) = __builtin_amdgcn_exp2f(Mw - Mg) / Lg;
    waitl();
    *(volatile LAS unsigned*)(L + L_SCRDY) = 1u;
    if (g == 0 && lane == 0) __hip_atomic_store((GAS u64*)(ws + W_GEN), tag, RLX_AGENT);
  };
  if (TLBPF && tw == 6) {
    const char* kb = (const char*)kc + chunk;
    const char* vb = (const char*)vc + chunk;
#pragma unroll 1
    for (int i = 1; i < 32; ++i) {
      if (TLBPF == 1) lds_wait<8>(L, L_FULL, max(4 * i - TLBAHEAD, 1));
      const char* a = (i < 16 ? kb : vb) + (i & 15) * 65536;
      unsigned t;
      asm volatile("s_load_dword %0, %1, 0x0\n\ts_waitcnt lgkmcnt(0)" : "=s"(t) : "s"(a) : "memory");
    }
  }
  if (tw == 5) {
    lds_wait<2>(L, L_FULL, LEADAT);
    *(volatile LAS unsigned*)(L + L_CORR) = *(volatile LAS unsigned*)(L + L_E) + ((gi >> 3) == 2 ? -QLAG : LEAD) * 128;
  }
  auto pace = [&]() {
    const unsigned tag32 = (unsigned)(tag >> 32);
    unsigned A = __builtin_amdgcn_readfirstlane(*(volatile LAS unsigned*)(L + L_CORR)), prev = 0;
    int Bd = 0, tk = 0, f = 0;
    const int adv = rb >= 0 ? ADVB : jc >= 0 ? ADVC : 0;
    auto run_until = [&](int tt) {
#pragma unroll 1
      while (f < tt) {
        const int nf = (int)lds_wait<8>(L, L_FULL, f + 1);
        f = max(nf, f + 1);
        *(volatile LAS unsigned*)(L + L_CORR) = A + Bd * (f - tk) - adv * f;
      }
    };
#pragma unroll 1
    for (int k = 0; k < NCP; ++k) {
      const int tc = CP0 + CPS * k;
      run_until(tc);
      const unsigned e = __builtin_amdgcn_readfirstlane(*(volatile LAS unsigned*)(L + L_E));
      if (lane == 0 && (g >> 5) == 7) __hip_atomic_store((GAS u64*)(ws + W_PROG) + k * 256 + (g & 31), ((u64)tag32 << 32) | e, RLX_AGENT);
      run_until(tc + CPD + (STAG ? (g >> 3) % STAG : 0));
      float sum = 0.f, cnt = 0.f;
      {
        const u64 pv = __hip_atomic_load((GAS u64*)(ws + W_PROG) + k * 256 + (lane & 31), RLX_AGENT);
        const bool ok = (unsigned)(pv >> 32) == tag32;
        sum += ok ? (float)(int)((unsigned)pv - e) : 0.f; cnt += ok ? 1.f : 0.f;
      }
      sum = wave_sum(sum); cnt = wave_sum(cnt);
      const unsigned eb = e + __builtin_amdgcn_readfirstlane((int)(sum / fmaxf(cnt, 1.f)));
      if (k) Bd = max(BLO, min(BHI, (int)(eb - prev) / CPS - BIAS));
      prev = eb; A = eb; tk = tc;
    }
    run_until(NSTEP - 6);
    *(volatile LAS unsigned*)(L + L_TFIN) = A + Bd * (NSTEP - tk) + NSTEP * TSCHED;
  };
  if (jc < 0) {
    if (tw == 5) pace();
    if (tw == 4) exchange();
  }
  auto wait_weights = [&](int step) {
    if (step > NSTEP) { lds_wait<8>(L, L_FULL, NSTEP - 2); lds_wait<1>(L, L_PUB, 4u); } else lds_wait<TWSLEEP>(L, L_FULL, step);
  };
  if (jc < 0) {
    if (rb < 0) {
      return;
    }
    if (tw >= 4) return;
    wait_weights(TWB + TWS * tw);
    u32x4 fr[2][8];
    {
      const __amdgpu_buffer_rsrc_t r = __builtin_amdgcn_make_buffer_rsrc((void*)wo, 0, HD * D * 2, 0x00020000);
      u32x4 raw[2][8];
      const int voff = tile_voff<D, 32, 8>(lane);
#pragma unroll
      for (int s = 0; s < 2; ++s) ldtile<D>(raw[s], r, (512 * ks + 128 * tw + 64 * s) * D + 64 * cs, voff);
#pragma unroll
      for (int s = 0; s < 2; ++s) mkfrag(raw[s], fr[s]);
    }
    unsigned off = W_RECA + (((kg & 1) * 16 + 2 * ks + (tw >> 1)) * 8 + (kg >> 1)) * RECA + 256 * (tw & 1) + 16 * n;
    unsigned ta = tw * 4352 + kg * 272 + 16 * n, tb = tw * 4352 + n * 272 + 16 * kg;
    unsigned wa = L_RED + ((tw * 2 + (lane >> 5)) * 64 + ccol(n) + 2 * (kg & 1)) * 4, ra = L_RED + lane * 8;
    int so = W_RECB + ((ks * 2 + (lane >> 5)) * D + 64 * cs + 2 * (lane & 31)) * 4;
    pin(ta); pin(tb);
    u32x4 v[4];
#pragma unroll
    for (int i = 0; i < 4; ++i) { v[i] = u32x4{SENT, SENT, SENT, SENT}; pin(v[i]); }
    pin(off); pin(wa); pin(ra); pin(so);
    lds_wait<4>(L, L_SCRDY, 1u);
    if (WARMC && tw == 3) { lds_wait<8>(L, L_FULL, WARMC); warm_code(lane); }
    lds_wait<1>(L, L_PUB, 4u);
    if (BX) {
      const unsigned tf = __builtin_amdgcn_readfirstlane(*(volatile LAS unsigned*)(L + L_TFIN)) + BX * 128;
#pragma unroll 1
      for (int i = 0; i < 64 && (int)(((unsigned)__builtin_amdgcn_s_memrealtime() << 7) - tf) < 0; ++i) __builtin_amdgcn_s_sleep(1);
    }
    gather4<1, SB>(v, off, ws, 2 * RECA);
    if (tw == 0) STY(6);
#pragma unroll
    for (int i = 0; i < 4; ++i) *LP(u32x4, ta + i * 1088) = v[i];
    waitl();
#pragma unroll
    for (int i = 0; i < 4; ++i) v[i] = *LP(u32x4, tb + i * 64);
    f32x4 d[4];
#pragma unroll
    for (int c = 0; c < 4; ++c) d[c] = mfma(v[0], fr[0][c], f32x4{0.f, 0.f, 0.f, 0.f});
#pragma unroll
    for (int s = 1; s < 4; ++s)
#pragma unroll
      for (int c = 0; c < 4; ++c) d[c] = mfma(v[s], fr[s >> 1][4 * (s & 1) + c], d[c]);
    float e[4];
#pragma unroll
    for (int c = 0; c < 4; ++c) e[c] = tr32(d[c][0] + d[c][2], d[c][1] + d[c][3]);
    *LP(f32x2, wa) = f32x2{tr16(e[0], e[2]), tr16(e[1], e[3])};
    bar();
    if (tw == 0) {
      const f32x2 s = (*LP(f32x2, ra) + *LP(f32x2, ra + 512)) + (*LP(f32x2, ra + 1024) + *LP(f32x2, ra + 1536));
      __builtin_amdgcn_raw_buffer_store_b64(u32x2{canon(s[0]), canon(s[1])}, rs, so, 0, AUX_SC1);
      STY(7);
    }
    return;
  }
#define TS(i) do {} while (0)
  u32x4 fg[2][8], fd[8];
  {
    const GAS char* gp = (const GAS char*)((n & 8) ? wu : wg) + ((64 * tw + 16 * kg) * FF + 64 * jc + 8 * (n & 7)) * 2;
    const __amdgpu_buffer_rsrc_t r3 = __builtin_amdgcn_make_buffer_rsrc((void*)wd, 0, FF * D * 2, 0x00020000);
    wait_weights(tw == 5 ? 6 : TWC + TWS * tw);
    u32x4 raw[2][8];
#pragma unroll
    for (int j = 0; j < 16; ++j) raw[j >> 3][j & 7] = __builtin_nontemporal_load((const GAS u32x4*)(gp + j * (FF * 2)));
    __builtin_amdgcn_sched_barrier(0);
    pairrows(raw[0], fg[0]); pin8(fg[0]);
    pairrows(raw[1], fg[1]); pin8(fg[1]);
    if (!WDLATE) { ldtile<D>(raw[0], r3, 64 * jc * D + 64 * tw, tile_voff<D, 32, 8>(lane)); pairrows(raw[0], fd); pin8(fd); }
    if (tw == 5) pace();
    if (tw == 4) exchange();
    if (WARMC && tw == 3) { lds_wait<8>(L, L_FULL, WARMC); warm_code(lane); warm_code(lane + 64); }
  }
  const int b = (n >> 2) & 1;
  const int col = 64 * tw + 16 * kg + 4 * (n & 3);
  const u32x2 xp = *(const GAS u32x2*)(x + b * D + col);
  f32x4 xf = {bflo(xp[0]), bfhi(xp[0]), bflo(xp[1]), bfhi(xp[1])};
  unsigned off = W_RECB + (((n & 8) + b) * D + col) * 4;
  unsigned a_h = L_H + (b * D + col) * 2, a_st = L_ST + (b * 8 + tw) * 8 + (lane >> 5) * 4;
  unsigned a_red = L_RED + tw * 1536 + (n >> 2) * 512 + (kg >> 1) * 256 + (kg & 1) * 16;
  unsigned a_rd = L_RED + tw * 512 + lane * 4, a_rc = L_RED + 1024 + lane * 4, a_sr = L_ST + (tw * 8 + (lane & 7)) * 8;
  unsigned a_g = L_G + (tw * 64 + 8 * (lane & 7) + (lane >> 3)) * 2, a_gr = L_G + (b * 64 + 32 * (n & 1) + 8 * kg) * 2;
  int so = lane < 32 ? W_RECC + ((jc * 2 + kg) * D + 64 * tw + ccol(n)) * 4 : OOB;
  if (!((0x111 >> n) & 1)) { a_red = tw * 2048 + lane * 16; a_st = 16384 + tw * 256 + lane * 4; }
  unsigned msel = (lane & 8) ? ~0u : 0u;
  u64 wr = __ballot((0x111 >> n) & 1);
  const int pg = lane >> 3, ch = lane & 7;
  unsigned offd = W_RECC + pg * (2 * D * 4) + (32 * jc + 4 * ch) * 4, a_hd = L_H + (32 * jc + 4 * ch + 2 * (lane >> 5)) * 2;
  int oo = (lane & 24) ? OOB : (32 * jc + 4 * ch + 2 * (lane >> 5)) * 2;
  u32x4 v[4], w[4], b0, b1;
  constexpr unsigned ONE2 = 0x3F803F80u;
  b0 = u32x4{ONE2, ONE2, ONE2, ONE2}; b1 = b0;
#pragma unroll
  for (int i = 0; i < 4; ++i) {
    v[i] = u32x4{SENT, SENT, SENT, SENT}; pin(v[i]);
  }
  pin(xf); pin(off); pin(a_h); pin(a_st); pin(a_red); pin(a_rd); pin(a_sr); pin(a_g); pin(a_gr); pin(so); pin(offd); pin(a_hd); pin(oo);
  pin(a_rc); pin(msel); pin(b0); pin(b1);
  const __amdgpu_buffer_rsrc_t ro = __builtin_amdgcn_make_buffer_rsrc((void*)out, 0, 2 * D * 2, 0x00020000);
  lds_wait<4>(L, L_SCRDY, 1u);
  if (WDLATE) {
    const __amdgpu_buffer_rsrc_t r3 = __builtin_amdgcn_make_buffer_rsrc((void*)wd, 0, FF * D * 2, 0x00020000);
    int vd = tile_voff<D, 32, 8>(lane);
    pin(vd);
    wait_weights(NSTEP + 1);
    u32x4 raw[8];
    ldtile<D>(raw, r3, 64 * jc * D + 64 * tw, vd);
    pairrows(raw, fd); pin8(fd);
  } else {
    lds_wait<1>(L, L_PUB, 4u);
  }
  if (tw == 0) ST(15);
  if (CX) {
    const unsigned tf = __builtin_amdgcn_readfirstlane(*(volatile LAS unsigned*)(L + L_TFIN)) + CX * 128;
#pragma unroll 1
    for (int i = 0; i < 64 && (int)(((unsigned)__builtin_amdgcn_s_memrealtime() << 7) - tf) < 0; ++i) __builtin_amdgcn_s_sleep(1);
  }
  gather4<RC, SC>(v, off, ws, 2 * D * 4);
  TS(5);
  if (tw == 0) STY(8);
  {
    f32x4 a = (__builtin_bit_cast(f32x4, v[0]) + __builtin_bit_cast(f32x4, v[1])) + (__builtin_bit_cast(f32x4, v[2]) + __builtin_bit_cast(f32x4, v[3]));
#pragma unroll
    for (int j = 0; j < 4; ++j) { float t = a[j] + dpp<ROR8>(a[j]); pin(t); a[j] = t; }
    const unsigned a01 = pk2(a[0], a[1]), a23 = pk2(a[2], a[3]);
    const unsigned h0 = pk2(xf[0] + bflo(a01), xf[1] + bfhi(a01)), h1 = pk2(xf[2] + bflo(a23), xf[3] + bfhi(a23));
#define ACT(o, v, ctrl) o = (unsigned)__builtin_amdgcn_update_dpp((int)(o), (int)(v), ctrl, 0xf, 0x3, false)
    ACT(b0[0], h0, 0x00); ACT(b0[1], h1, 0x00); ACT(b0[2], h0, 0x55); ACT(b0[3], h1, 0x55);
    ACT(b1[0], h0, 0xAA); ACT(b1[1], h1, 0xAA); ACT(b1[2], h0, 0xFF); ACT(b1[3], h1, 0xFF);
    f32x4 d[8];
#pragma unroll
    for (int c = 0; c < 8; ++c) {
      d[c] = mfma(fg[1][c], b1, mfma(fg[0][c], b0, f32x4{0.f, 0.f, 0.f, 0.f}));
      if (c) *LP(f32x4, a_red + (c - 1) * 32) = d[c - 1];
      __builtin_amdgcn_sched_group_barrier(0x8, 2, 0); __builtin_amdgcn_sched_group_barrier(0x2, 3, 0); __builtin_amdgcn_sched_group_barrier(0x200, 1, 0);
    }
    *LP(f32x4, a_red + 7 * 32) = d[7];
    float s1 = dot2(h1, ONE2, dot2(h0, ONE2, 0.f)), s2 = dot2(h1, h1, dot2(h0, h0, 0.f));
    s1 = x16_sum(tr32(s1, s2));
    s1 += dpp<XOR1>(s1); s1 += dpp<XOR2>(s1);
    *LP(u32x2, a_h) = u32x2{h0, h1};
    *LP(float, a_st) = s1;
  }
  TS(6);
  bar();
  TS(7);
  if (tw == 0) STY(12);
  if (tw < 2) {
    f32x2 st = *LP(f32x2, a_sr);
    f32x2 ps[7], pc[7];
#pragma unroll
    for (int i = 0; i < 7; ++i) {
      ps[i] = f32x2{*LP(float, a_rd + i * 1536), *LP(float, a_rd + i * 1536 + 256)};
      pc[i] = f32x2{*LP(float, a_rc + i * 1536), *LP(float, a_rc + i * 1536 + 256)};
    }
    __builtin_amdgcn_sched_barrier(0);
#pragma unroll
    for (int j = 0; j < 2; ++j) { st[j] += dpp<XOR1>(st[j]); st[j] += dpp<XOR2>(st[j]); st[j] += dpp<HMIR>(st[j]); }
    const float mean = st[0] * (1.f / D);
    const float var = fmaxf(fmaf(-mean, mean, st[1] * (1.f / D)), 0.f);
    const float mb = rbf(mean);
    const float rsd = __builtin_amdgcn_rcpf(rbf(__builtin_amdgcn_sqrtf(rbf(rbf(var) + 1e-5f))));
    const f32x2 sm = ((ps[0] + ps[1]) + (ps[2] + ps[3])) + ((ps[4] + ps[5]) + ps[6]);
    const f32x2 cm = ((pc[0] + pc[1]) + (pc[2] + pc[3])) + ((pc[4] + pc[5]) + pc[6]);
    const unsigned gu = pk2((sm[0] - mb * cm[0]) * rsd, (sm[1] - mb * cm[1]) * rsd);
    const float xg = bflo(gu);
    constexpr float K1 = -2.f * 0.7978845608028654f * 1.4426950408889634f;
    const float ge = xg * __builtin_amdgcn_rcpf(1.f + __builtin_amdgcn_exp2f(xg * fmaf(xg * xg, K1 * 0.044715f, K1)));
    *LP(unsigned short, a_g) = (unsigned short)f2bf(rbf(ge) * bfhi(gu));
  }
  drain4<RC>(v);
#pragma unroll
  for (int i = 0; i < 4; ++i) {
    const unsigned iv = (i < 3 || pg < 4) ? SENT : 0u;
    if (tw == 6) { w[i] = u32x4{iv, iv, iv, iv}; pin(w[i]); }
  }
  bar();
  if (tw == 0) STY(13);
  {
    const u32x4 a0 = *LP(u32x4, a_gr);
    f32x4 d[8];
#pragma unroll
    for (int c = 0; c < 8; ++c) d[c] = mfma(a0, fd[c], f32x4{0.f, 0.f, 0.f, 0.f});
    u32x4 o;
#pragma unroll
    for (int c = 0; c < 4; ++c) {
      const unsigned keep = (__float_as_uint(d[c + 4][1]) & msel) | (__float_as_uint(d[c][0]) & ~msel);
      const unsigned send = (__float_as_uint(d[c][1]) & msel) | (__float_as_uint(d[c + 4][0]) & ~msel);
      o[c] = canon(__uint_as_float(keep) + dpp<ROR8>(__uint_as_float(send)));
    }
    __builtin_amdgcn_raw_buffer_store_b128(o, rs, so, 0, AUX_SC1);
    if (tw == 0) ST(9);
  }
  if (tw == 6) {
    const unsigned hv = *LP(unsigned, a_hd);
    if (DDELAY) __builtin_amdgcn_s_sleep(DDELAY);
    gather4<RD, SD>(w, offd, ws, 8 * (2 * D * 4));
    ST(10);
    const f32x4 t = (__builtin_bit_cast(f32x4, w[0]) + __builtin_bit_cast(f32x4, w[1])) + (__builtin_bit_cast(f32x4, w[2]) + __builtin_bit_cast(f32x4, w[3]));
    float s0 = tr32(t[0], t[2]), s1 = tr32(t[1], t[3]);
    s0 = x16_sum(s0); s1 = x16_sum(s1);
    s0 += dpp<ROR8>(s0); s1 += dpp<ROR8>(s1);
    const unsigned dn = pk2(s0, s1);
    __builtin_amdgcn_raw_buffer_store_b32(pk2(bflo(hv) + bflo(dn), bfhi(hv) + bfhi(dn)), ro, oo, 0, 0);
    drain4<RD>(w);
    ST(11);
  }
}

extern "C" void kernel_launch(void* const* d_in, const int* in_sizes, int n_in, void* d_out, int out_size, void* d_ws,
                              size_t ws_size, hipStream_t stream) {
  (void)hipFuncSetAttribute((const void*)fused_block, hipFuncAttributeMaxDynamicSharedMemorySize, LDS_BYTES);
  hipLaunchKernelGGL(fused_block, dim3(NWG), dim3(NTHR), LDS_BYTES, stream, (const bf16_t*)d_in[0], (const bf16_t*)d_in[1],
                     (const bf16_t*)d_in[2], (const bf16_t*)d_in[3], (const bf16_t*)d_in[4], (const bf16_t*)d_in[6],
                     (const bf16_t*)d_in[7], (const bf16_t*)d_in[8], (bf16_t*)d_out, (unsigned char*)d_ws + (WSEND ? ((ws_size - (8u << 20)) & ~(size_t)4095) : 0));
}
```

```cpp
#include <hip/hip_runtime.h>
#include <hip/hip_bf16.h>
#include <stdint.h>
#include <type_traits>

typedef unsigned short bf16_t;
typedef unsigned long long u64;
typedef unsigned u32x4 __attribute__((ext_vector_type(4)));
typedef unsigned u32x2 __attribute__((ext_vector_type(2)));
typedef float f32x4 __attribute__((ext_vector_type(4)));
typedef float f32x2 __attribute__((ext_vector_type(2)));
typedef __bf16 bf16x2 __attribute__((ext_vector_type(2)));
typedef __bf16 bf16x8 __attribute__((ext_vector_type(8)));
#define LAS __attribute__((address_space(3)))
#define GAS __attribute__((address_space(1)))
#define RLX_AGENT __ATOMIC_RELAXED, __HIP_MEMORY_SCOPE_AGENT
#define RLX_WG __ATOMIC_RELAXED, __HIP_MEMORY_SCOPE_WORKGROUP

constexpr int D = 448, SK = 16384, DH = 256, HD = 4096, FF = 1792;
constexpr int NWG = 256, NTHR = 768;
constexpr int NPH = 64, NSTEP = 2 * NPH;
constexpr int NSLOT = 8, SLOT_BYTES = 16384;
#define W0 19
#define WMIN 15
#define WMAX 32
#define TSCHED 6940
#define LEAD 0
#define NCP 4
#define CP0 14
#define CPS 30
#define CPD 6
#define PMODE 1
#define STAG 8
#define PF_STEP 0
#define CWDELAY 0
#define TWSLEEP 2
#define RB 1
#define SB 0
#define RC 1
#define SC 0
#define RD 1
#define SD 0
#define WDLATE 1
#define JUNK 1
#define CWAIT 0
#define WARMC 120
#define EARLYT 1
#define VTOUCH 0
#define BLO -1000
#define BHI 1000
#define NPRO 2
#define QFIRST 1
#define PAST 0
#define CODEPF 20480
#define QPOLL 5
#define QSLEEP 4
#define LEADAT 3
#define FASTPRO 1
#define QLAG 150
#define MAPX 0
#define TLBPF 0
#define TLBAHEAD 10
#define EARLYINV 0
#define WSEND 0
#define INVAT 0
#define XSTAG 0
#define RSTAG 0
#define CX 245
#define BX 140
#define BIAS 40
#define WSTART 22
#define ADVB 0
#define ADVC 40
#define GAIN 8
#define PRE 9
#define TWB 24
#define TWC 36
#define TWS 0
#define ML_STEP 96
#define LPRIO 3
#define CSLEEP 1
#define VSLEEP 1
#define BDELAY 0
#define CDELAY 0
#define DDELAY 6
#define CXCD 7
constexpr int L_CTL = NSLOT * SLOT_BYTES;
constexpr int L_FREE = L_CTL, L_QCNT = L_CTL + 20, L_QRDY = L_CTL + 24, L_C1 = L_CTL + 28, L_C2 = L_CTL + 32,
              L_SCRDY = L_CTL + 36, L_SCALE = L_CTL + 40, L_PUB = L_CTL + 44, L_CORR = L_CTL + 48, L_TFIN = L_CTL + 52, L_MW = L_CTL + 64, L_LW = L_CTL + 80;
constexpr int L_FULL = L_CTL + 128;
constexpr int L_E = L_FULL + 256;
constexpr int L_QX = L_E + 256;
constexpr int L_Q = L_QX;
constexpr int L_P = L_Q + 512;
constexpr int L_ZERO = L_P + 8192;
constexpr int L_RED = L_ZERO + 128;
constexpr int L_QPART = L_RED + 1024;
constexpr int L_ST = L_RED + 10752;
constexpr int L_H = L_ST + 128;
constexpr int L_G = L_H + 1792;
constexpr int L_DBG = L_G + 256;
constexpr int LDS_BYTES = L_DBG + 256;
static_assert(LDS_BYTES <= 160 * 1024, "LDS");
constexpr int W_GEN = 0;
constexpr int W_FLAG = 4096;
constexpr int W_ML = 8192;
constexpr int W_PROG = 12288;
constexpr int W_QG = W_PROG + 16384;
constexpr int W_RECA = W_QG + 65536, RECA = 512;
constexpr int W_RECB = W_RECA + 256 * RECA;
constexpr int W_RECC = W_RECB + 8 * 2 * D * 4;
constexpr int W_END = W_RECC + 28 * 2 * D * 4;
constexpr long long W_STAMP = (1 << 22) - (WSEND ? (1ll << 30) - (8 << 20) : 0);
constexpr unsigned SENT = 0xFFFFFFFFu;
constexpr int OOB = 0x40000000;
constexpr int AUX_SC1 = 16, AUX_NT = 2;
constexpr unsigned SPIN_CAP = 1u << 20;

__device__ __forceinline__ float bflo(unsigned v) { return __builtin_bit_cast(float, v << 16); }
__device__ __forceinline__ float bfhi(unsigned v) { return __builtin_bit_cast(float, v & 0xffff0000u); }
__device__ __forceinline__ unsigned f2bf(float f) { return __builtin_bit_cast(unsigned short, (__bf16)f); }
__device__ __forceinline__ float rbf(float f) { return bflo(f2bf(f)); }
__device__ __forceinline__ unsigned pk2(float lo, float hi) {
  bf16x2 v = {(__bf16)lo, (__bf16)hi};
  return __builtin_bit_cast(unsigned, v);
}
__device__ __forceinline__ float dot2(unsigned k, unsigned q, float acc) {
  return __builtin_amdgcn_fdot2_f32_bf16(__builtin_bit_cast(bf16x2, k), __builtin_bit_cast(bf16x2, q), acc, false);
}
template <int CTRL> __device__ __forceinline__ unsigned dppu(unsigned x) { return __builtin_amdgcn_mov_dpp((int)x, CTRL, 0xf, 0xf, true); }
template <int CTRL> __device__ __forceinline__ float dpp(float x) { return __uint_as_float(dppu<CTRL>(__float_as_uint(x))); }
constexpr int XOR1 = 0xB1, XOR2 = 0x4E, HMIR = 0x141, MIR = 0x140, ROR4 = 0x124, ROR8 = 0x128, SHL = 0x100;
__device__ __forceinline__ float row16_sum(float x) {
  x += dpp<XOR1>(x); x += dpp<XOR2>(x); x += dpp<HMIR>(x); x += dpp<MIR>(x);
  return x;
}
__device__ __forceinline__ float tr32(float a, float b) {
  auto s = __builtin_amdgcn_permlane32_swap(__float_as_uint(a), __float_as_uint(b), false, false);
  return __uint_as_float(s[0]) + __uint_as_float(s[1]);
}
__device__ __forceinline__ float tr16(float a, float b) {
  auto s = __builtin_amdgcn_permlane16_swap(__float_as_uint(a), __float_as_uint(b), false, false);
  return __uint_as_float(s[0]) + __uint_as_float(s[1]);
}
__device__ __forceinline__ float x16_sum(float x) { return tr16(x, x); }
__device__ __forceinline__ float x32_sum(float x) { return tr32(x, x); }
__device__ __forceinline__ float x16_max(float x) {
  auto s = __builtin_amdgcn_permlane16_swap(__float_as_uint(x), __float_as_uint(x), false, false);
  return fmaxf(__uint_as_float(s[0]), __uint_as_float(s[1]));
}
__device__ __forceinline__ float x32_max(float x) {
  auto s = __builtin_amdgcn_permlane32_swap(__float_as_uint(x), __float_as_uint(x), false, false);
  return fmaxf(__uint_as_float(s[0]), __uint_as_float(s[1]));
}
__device__ __forceinline__ float wave_sum(float x) { return x32_sum(x16_sum(row16_sum(x))); }
__device__ __forceinline__ unsigned canonu(unsigned u) { return min(u, SENT - 1u); }
__device__ __forceinline__ unsigned canon(float f) { return canonu(__float_as_uint(f)); }
__device__ __forceinline__ f32x4 mfma(u32x4 a, u32x4 b, f32x4 c) {
  return __builtin_amdgcn_mfma_f32_16x16x32_bf16(__builtin_bit_cast(bf16x8, a), __builtin_bit_cast(bf16x8, b), c, 0, 0, 0);
}
template <int N> __device__ __forceinline__ void waitv() { asm volatile("s_waitcnt vmcnt(%0)" ::"i"(N) : "memory"); }
__device__ __forceinline__ void waitl() { asm volatile("s_waitcnt lgkmcnt(0)" ::: "memory"); }
__device__ __forceinline__ void bar() { waitl(); __builtin_amdgcn_s_barrier(); asm volatile("" ::: "memory"); }
__device__ __forceinline__ u64 mix64(u64 z) {
  z += 0x9E3779B97F4A7C15ull; z = (z ^ (z >> 30)) * 0xBF58476D1CE4E5B9ull; z = (z ^ (z >> 27)) * 0x94D049BB133111EBull;
  return z ^ (z >> 31);
}
#define STR2(x) #x
#define STR(x) STR2(x)
#define NTS " nt"
#define BIGINC "0xe800"
#define BIGHI ""
#define DMA4(a, b, c, d) \
  "global_load_lds_dwordx4 %[" #a "], s[60:61] offset:0" NTS "\n\t" \
  "global_load_lds_dwordx4 %[" #b "], s[60:61] offset:1024" NTS "\n\t" \
  "global_load_lds_dwordx4 %[" #c "], s[60:61] offset:2048" NTS "\n\t" \
  "global_load_lds_dwordx4 %[" #d "], s[60:61] offset:3072" NTS "\n\t"
__device__ __forceinline__ void stream_steps(const unsigned (&v)[16], const char*& p, int& t, int tend, int& w, int& pub, int& sch,
                                             unsigned a_free, unsigned a_full) {
  unsigned vf, vt, ve;
  asm volatile(
      "s_mov_b64 s[60:61], %[p]\n\t"
      "s_getpc_b64 s[62:63]\n"
      "0:\n\t"
      "s_add_u32 s62, s62, 9f-0b-8*" STR(WMIN) "\n\t"
      "s_addc_u32 s63, s63, 0\n\t"
      "s_mov_b32 s73, 0x100000\n"
      "1:\n\t"
      "s_memrealtime s[64:65]\n\t"
      "ds_read_b32 %[vf], %[af]\n\t"
      "s_lshl_b32 s66, %[w], 3\n\t"
      "s_add_u32 s70, s62, s66\n\t"
      "s_addc_u32 s71, s63, 0\n\t"
      "s_lshl_b32 s66, %[t], 14\n\t"
      "s_and_b32 m0, s66, 0x1c000\n\t"
      "s_swappc_b64 s[68:69], s[70:71]\n\t"
      DMA4(v0, v1, v2, v3)
      "s_add_u32 m0, m0, 0x1000\n\t"
      "s_swappc_b64 s[68:69], s[70:71]\n\t"
      DMA4(v4, v5, v6, v7)
      "s_add_u32 m0, m0, 0x1000\n\t"
      "s_swappc_b64 s[68:69], s[70:71]\n\t"
      DMA4(v8, v9, v10, v11)
      "s_add_u32 m0, m0, 0x1000\n\t"
      "s_swappc_b64 s[68:69], s[70:71]\n\t"
      DMA4(v12, v13, v14, v15)
      "s_lshl_b32 s66, %[t], 4\n\t"
      "s_add_i32 s66, s66, 12\n\t"
      "s_sub_i32 s66, s66, %[w]\n\t"
      "s_ashr_i32 s66, s66, 4\n\t"
      "s_max_i32 %[pub], %[pub], s66\n\t"
      "v_mov_b32 %[vt], %[pub]\n\t"
      "s_waitcnt lgkmcnt(0)\n\t"
      "v_readlane_b32 s65, %[vf], 63\n\t"
      "s_lshl_b32 s64, s64, 7\n\t"
      "s_sub_i32 s64, s64, %[sch]\n\t"
      "v_mov_b32 %[ve], s64\n\t"
      "s_add_i32 %[sch], %[sch], " STR(TSCHED) "\n\t"
      "ds_write2_b32 %[al], %[vt], %[ve] offset1:64\n\t"
      "s_sub_i32 s64, s64, s65\n\t"
      "s_ashr_i32 s64, s64, 4\n\t"
      "s_mulk_i32 s64, " STR(GAIN) "\n\t"
      "s_ashr_i32 s64, s64, 11\n\t"
      "s_add_i32 s64, s64, " STR(W0) "\n\t"
      "s_max_i32 s64, s64, " STR(WMIN) "\n\t"
      "s_min_i32 %[w], s64, " STR(WMAX) "\n\t"
      "s_and_b32 s66, %[t], 3\n\t"
      "s_movk_i32 s67, 0x800\n\t"
      "s_mov_b32 s72, 0\n\t"
      "s_cmp_eq_u32 s66, 3\n\t"
      "s_cmov_b32 s67, " BIGINC "\n\t"
      BIGHI
      "s_add_u32 s60, s60, s67\n\t"
      "s_addc_u32 s61, s61, s72\n\t"
      "s_add_i32 %[t], %[t], 1\n\t"
      "s_add_i32 s66, %[t], -7\n"
      "2:\n\t"
      "v_cmp_gt_i32 s[64:65], s66, %[vf]\n\t"
      "s_cmp_lg_u32 s64, 0\n\t"
      "s_cbranch_scc1 3f\n"
      "4:\n\t"
      "s_cmp_lg_u32 %[t], %[tend]\n\t"
      "s_cbranch_scc1 1b\n\t"
      "s_branch 8f\n"
      "3:\n\t"
      "s_cmp_eq_u32 s73, 0\n\t"
      "s_cbranch_scc1 4b\n\t"
      "s_sub_u32 s73, s73, 1\n\t"
      "ds_read_b32 %[vf], %[af]\n\t"
      "s_waitcnt lgkmcnt(0)\n\t"
      "s_branch 2b\n"
      "9:\n\t"
      ".set i_, " STR(WMIN) "\n\t.rept " STR(WMAX) "-" STR(WMIN) "+1\n\ts_waitcnt vmcnt(i_)\n\ts_setpc_b64 s[68:69]\n\t.set i_, i_+1\n\t.endr\n"
      "8:\n\t"
      "s_mov_b64 %[p], s[60:61]"
      : [p] "+s"(p), [t] "+s"(t), [w] "+s"(w), [pub] "+s"(pub), [sch] "+s"(sch), [vf] "=&v"(vf), [vt] "=&v"(vt), [ve] "=&v"(ve)
      : [tend] "s"(tend), [af] "v"(a_free), [al] "v"(a_full),
        [v0] "v"(v[0]), [v1] "v"(v[1]), [v2] "v"(v[2]), [v3] "v"(v[3]), [v4] "v"(v[4]), [v5] "v"(v[5]), [v6] "v"(v[6]), [v7] "v"(v[7]),
        [v8] "v"(v[8]), [v9] "v"(v[9]), [v10] "v"(v[10]), [v11] "v"(v[11]), [v12] "v"(v[12]), [v13] "v"(v[13]), [v14] "v"(v[14]), [v15] "v"(v[15])
      : "memory", "scc", "s60", "s61", "s62", "s63", "s64", "s65", "s66", "s67", "s68", "s69", "s70", "s71", "s72", "s73");
}
static_assert(WMAX <= 44 && WMIN >= 13 && WMIN <= W0 && W0 <= WMAX && WMIN <= WSTART && WSTART <= WMAX, "window");
template <int SLEEP> __device__ __forceinline__ unsigned lds_wait(LAS unsigned char* L, int off, unsigned want) {
  unsigned v = 0;
#pragma unroll 1
  for (unsigned s = 0; s < SPIN_CAP; ++s) {
    v = __builtin_amdgcn_readfirstlane(*(volatile LAS unsigned*)(L + off));
    if (v >= want) break;
    if (SLEEP) __builtin_amdgcn_s_sleep(SLEEP);
  }
  asm volatile("" ::: "memory");
  return v;
}
__device__ __forceinline__ int ccol(int n) { return 8 * (n & 7) + 4 * (n >> 3); }
template <int LD, int RS, int RK> __device__ __forceinline__ int tile_voff(int lane) { return ((RS * ((lane >> 3) & 1) + RK * (lane >> 4)) * LD + 8 * (lane & 7)) * 2; }
template <int LD> __device__ __forceinline__ void ldtile(u32x4 (&raw)[8], __amdgpu_buffer_rsrc_t r, int elem0, int voff) {
#pragma unroll
  for (int j = 0; j < 8; ++j) raw[j] = __builtin_amdgcn_raw_buffer_load_b128(r, voff, (elem0 + j * LD) * 2, AUX_NT);
}
__device__ __forceinline__ void pairrows(const u32x4 (&raw)[8], u32x4 (&f)[8]) {
#pragma unroll
  for (int jp = 0; jp < 4; ++jp)
#pragma unroll
    for (int d = 0; d < 4; ++d) {
      f[2 * d][jp] = __builtin_amdgcn_perm(raw[2 * jp + 1][d], raw[2 * jp][d], 0x05040100u);
      f[2 * d + 1][jp] = __builtin_amdgcn_perm(raw[2 * jp + 1][d], raw[2 * jp][d], 0x07060302u);
    }
}
__device__ __forceinline__ void pin8(u32x4 (&f)[8]) {
  asm volatile("" : "+v"(f[0]), "+v"(f[1]), "+v"(f[2]), "+v"(f[3]), "+v"(f[4]), "+v"(f[5]), "+v"(f[6]), "+v"(f[7]));
}
__device__ __forceinline__ void mkfrag(const u32x4 (&raw)[8], u32x4 (&f)[8]) {
  u32x4 p[8];
  pairrows(raw, p);
#pragma unroll
  for (int c = 0; c < 4; ++c)
#pragma unroll
    for (int jp = 0; jp < 4; ++jp) {
      f[c][jp] = __builtin_amdgcn_update_dpp((int)p[c][jp], (int)p[4 + c][jp], ROR8, 0xf, 0xc, false);
      f[4 + c][jp] = __builtin_amdgcn_update_dpp((int)p[4 + c][jp], (int)p[c][jp], ROR8, 0xf, 0x3, false);
    }
  pin8(f);
}
__device__ __forceinline__ void mld4(u32x4& v, u64 m, unsigned voff, const unsigned char* sb) {
  asm volatile("s_mov_b64 exec, %1\n\tglobal_load_dwordx4 %0, %2, %3 sc1\n\ts_mov_b64 exec, -1" : "+v"(v) : "s"(m), "v"(voff), "s"(sb) : "memory");
}
__device__ __forceinline__ void mld2(u32x2& v, u64 m, unsigned voff, const unsigned char* sb) {
  asm volatile("s_mov_b64 exec, %1\n\tglobal_load_dwordx2 %0, %2, %3 sc1\n\ts_mov_b64 exec, -1" : "+v"(v) : "s"(m), "v"(voff), "s"(sb) : "memory");
}
__device__ __forceinline__ u64 inval4(u32x4 v) { return __ballot(max(max(v[0], v[1]), max(v[2], v[3])) == SENT); }
__device__ __forceinline__ u64 inval2(u32x2 v) { return __ballot(max(v[0], v[1]) == SENT); }

#define ST(i) do {} while (0)
#define STY(i) ST(i)

template <typename T> __device__ __forceinline__ void pin(T& v) { asm volatile("" : "+v"(v)); }
typedef short s16x4 __attribute__((ext_vector_type(4)));
__device__ __forceinline__ u32x2 tr_read(LAS unsigned char* p) {
  return __builtin_bit_cast(u32x2, __builtin_amdgcn_ds_read_tr16_b64_v4i16((LAS s16x4*)p));
}
template <int R, int SLP> __device__ __forceinline__ void gather4(u32x4 (&v)[4], unsigned off, const unsigned char* base, int stride) {
  u64 mk[4];
#pragma unroll
  for (int i = 0; i < 4; ++i) mk[i] = inval4(v[i]);
#pragma unroll 1
  for (unsigned spins = 0; spins < SPIN_CAP; ++spins) {
#pragma unroll
    for (int i = 0; i < 4; ++i) mld4(v[i], mk[i], off, base + i * stride);
    if (SLP) __builtin_amdgcn_s_sleep(SLP);
    asm volatile("s_waitcnt vmcnt(%4)" : "+v"(v[0]), "+v"(v[1]), "+v"(v[2]), "+v"(v[3]) : "i"(4 * (R - 1)) : "memory");
#pragma unroll
    for (int i = 0; i < 4; ++i) mk[i] = inval4(v[i]);
    if (((mk[0] | mk[1]) | (mk[2] | mk[3])) == 0) break;
  }
}
template <int R> __device__ __forceinline__ void drain4(u32x4 (&v)[4]) {
  if (R > 1) asm volatile("s_waitcnt vmcnt(0)" : "+v"(v[0]), "+v"(v[1]), "+v"(v[2]), "+v"(v[3])::"memory");
}
__device__ __forceinline__ void warm_code(int lane) {
  u64 pc;
  asm volatile("s_getpc_b64 %0" : "=s"(pc));
  const unsigned t = *(const GAS unsigned*)((pc & ~63ull) + lane * 64);
  asm volatile("" ::"v"(t));
}
#define LP(T, a) ((LAS T*)(size_t)(a))
__device__ __forceinline__ int role_b(int g) {
  const int xc = g & 7, i = g >> 3;
  if (CXCD == 1) return xc < 7 && i < 7 ? xc * 8 + i : (xc == 0 && i < 14 ? 56 + i - 7 : -1);
  if (CXCD == 3) return xc < 6 && i < 7 ? xc * 8 + i : (xc < 2 && i < 14 ? (6 + xc) * 8 + i - 7 : -1);
  if (CXCD == 5) return xc < 4 && i < 14 ? (i < 7 ? xc * 8 + i : (4 + xc) * 8 + i - 7) : -1;
  return i < 7 ? xc * 8 + i : -1;
}
__device__ __forceinline__ int role_c(int g) {
  const int xc = g & 7, i = g >> 3;
  if (CXCD == 1) return xc == 7 && i < 28 ? i : -1;
  if (CXCD == 3) return xc >= 6 && i < 14 ? (xc - 6) * 14 + i : -1;
  if (CXCD == 5) return xc >= 4 && i < 7 ? (xc - 4) * 7 + i : -1;
  if (CXCD == 7) return xc < 7 && i >= 7 && i < 11 ? 4 * xc + i - 7 : -1;
  if (CXCD == 8) return i >= 7 && (i - 7) * 8 + xc < 28 ? (i - 7) * 8 + xc : -1;
  if (CXCD == 2) return xc >= 6 && i >= 7 && i < 21 ? (xc - 6) * 14 + i - 7 : -1;
  return xc >= 4 && i >= 7 && i < 14 ? (xc - 4) * 7 + i - 7 : -1;
}

__global__ void __launch_bounds__(NTHR, 3)
fused_block(const bf16_t* __restrict__ x, const bf16_t* __restrict__ kc, const bf16_t* __restrict__ vc,
            const bf16_t* __restrict__ wq, const bf16_t* __restrict__ wo, const bf16_t* __restrict__ wg,
            const bf16_t* __restrict__ wu, const bf16_t* __restrict__ wd, bf16_t* __restrict__ out, unsigned char* ws) {
  extern __shared__ __attribute__((aligned(16))) unsigned char smem[];
  LAS unsigned char* L = (LAS unsigned char*)smem;
  const int tid = threadIdx.x;
  if (FASTPRO && MAPX == 0) {
    unsigned vv;
    asm volatile(
        "s_load_dwordx2 s[60:61], %[ka], 0x8\n\t"
        "v_readfirstlane_b32 s62, %[tid]\n\t"
        "v_lshlrev_b32 %[vv], 4, %[tid]\n\t"
        "s_lshr_b32 s63, %[g], 6\n\t"
        "s_cmp_eq_u32 s63, 2\n\t"
        "s_cselect_b32 s62, 64, s62\n\t"
        "s_cmp_gt_u32 s62, 63\n\t"
        "s_cbranch_scc1 2f\n\t"
        "s_lshr_b32 s63, %[g], 3\n\t"
        "s_add_u32 s63, s63, %[g]\n\t"
        "s_and_b32 s63, s63, 7\n\t"
        "s_andn2_b32 s62, %[g], 7\n\t"
        "s_or_b32 s62, s62, s63\n\t"
        "s_lshl_b32 s62, s62, 20\n\t"
        "s_mov_b32 m0, 0\n\t"
        "s_movk_i32 s63, 0x2000\n\t"
        "s_waitcnt lgkmcnt(0)\n\t"
        "s_add_u32 s60, s60, s62\n\t"
        "s_addc_u32 s61, s61, 0\n"
        "1:\n\t"
        "global_load_lds_dwordx4 %[vv], s[60:61]" NTS "\n\t"
        "s_addk_i32 m0, 0x400\n\t"
        "s_and_b32 s62, m0, 0x1c00\n\t"
        "s_cselect_b32 s62, s63, 0xffff2400\n\t"
        "v_add_u32 %[vv], s62, %[vv]\n\t"
        "s_bitcmp0_b32 m0, 15\n\t"
        "s_cbranch_scc1 1b\n"
        "2:\n\t"
        "s_waitcnt lgkmcnt(0)"
        : [vv] "=&v"(vv)
        : [ka] "s"(__builtin_amdgcn_kernarg_segment_ptr()), [tid] "v"(tid), [g] "s"(blockIdx.x)
        : "memory", "scc", "s60", "s61", "s62", "s63");
  }
  const int lane = tid & 63, wave = __builtin_amdgcn_readfirstlane(tid >> 6);
  const int g = blockIdx.x;
  const int gi = g >> 3;
  const int gl = MAPX == 0 ? (g & ~7) | ((g + gi) & 7) : MAPX == 1 ? (g & 7) * 32 + gi : ((gi >> 1) * 8 + ((g + (gi >> 1)) & 7)) * 2 + (gi & 1);
  const int bh = gl >> 3;

  const size_t chunk = (size_t)gl << 20;
  if (CODEPF && wave != 0) {
    u64 pc;
    const unsigned co = min((unsigned)(tid - 64) * 64u, (unsigned)CODEPF - 64u);
    asm volatile("s_getpc_b64 %0\n\ts_mov_b32 m0, %2\n\ts_nop 0\n\tglobal_load_lds_dword %1, %0" : "=&s"(pc) : "v"(co), "s"(L_RED) : "memory");
  }
  int wave0 = wave;
  asm volatile("" : "+s"(wave0));
  if (wave0 == 0) {
    __builtin_amdgcn_s_setprio(LPRIO);
    unsigned vo[16];
#pragma unroll
    for (int i = 0; i < 16; ++i) vo[i] = lane * 16 + (i & 7) * 8192 + (i >> 3) * 1024 - (i & 3) * 1024;
    unsigned a_free = lane < 32 ? L_FREE + (lane & 3) * 4 : L_CORR, a_full = L_FULL + lane * 4;
#pragma unroll
    for (int i = 0; i < 16; ++i) pin(vo[i]);
    pin(a_free); pin(a_full);
    __builtin_amdgcn_sched_barrier(0);
    const bool qp = (gi >> 3) == 2;
    const int ts = FASTPRO && MAPX == 0 && !qp ? 2 : 0;
    const char* p = (const char*)kc + chunk + ts * 2048;
    int t = ts, pub = 0, w = qp ? WMIN : WSTART, sch = ts * TSCHED;
    {
      const unsigned e0 = ((unsigned)__builtin_amdgcn_s_memrealtime() + (qp ? 1000 : -PAST)) << 7;
      *LP(unsigned, L_CTL + lane * 4) = lane == (L_CORR - L_CTL) / 4 ? e0 : 0u;
    }
    bar();
#pragma unroll 1
    for (int tend = NPH; tend <= NSTEP; tend += NPH) {
      stream_steps(vo, p, t, tend, w, pub, sch, a_free, a_full);
      p = (const char*)vc + chunk;
#pragma unroll
      for (int i = 0; i < 16; ++i) vo[i] ^= (lane >> 5) << 7;
    }
    volatile LAS int* vfull = (volatile LAS int*)(L + L_FULL) + lane;
    waitv<32>(); *vfull = NSTEP - 2;
    waitv<16>(); *vfull = NSTEP - 1;
    waitv<0>();  *vfull = NSTEP;
    ST(3);
    asm volatile("s_endpgm" ::: "memory");
  }
  if (wave == 1 && lane < 32) { ((LAS unsigned*)(L + L_ZERO))[lane] = 0u; ((LAS unsigned*)(L + L_ST))[lane] = 0u; }
  if (wave <= 4 || !QFIRST) bar();
  const __amdgpu_buffer_rsrc_t rs = __builtin_amdgcn_make_buffer_rsrc(ws, 0, W_END, 0x00020000);
  const int n = lane & 15, kg = lane >> 4;
  if (wave <= 4) {
    const int ci = wave - 1;
    unsigned full = 0;
    volatile LAS unsigned* vfree = (volatile LAS unsigned*)(L + L_FREE) + ci;
    {
      const int lo = (8 * ci + kg) * 512 + 16 * n;
      auto fetch = [&](int t, u32x4 (&d)[4]) {
        if ((int)full <= t) full = lds_wait<CSLEEP>(L, L_FULL, t + 1);
        LAS unsigned char* sb = L + (t & (NSLOT - 1)) * SLOT_BYTES + lo;
        d[0] = *(LAS u32x4*)(sb); d[1] = *(LAS u32x4*)(sb + 256); d[2] = *(LAS u32x4*)(sb + 2048); d[3] = *(LAS u32x4*)(sb + 2304);
        waitl();
        *vfree = t + 1;
        asm volatile("" : "+v"(d[0]), "+v"(d[1]), "+v"(d[2]), "+v"(d[3])::"memory");
      };
      u32x4 pre[PRE][4];
#pragma unroll
      for (int t = 0; t < PRE; ++t) fetch(t, pre[t]);
      lds_wait<1>(L, L_QRDY, 1u);
      const u32x4 q0 = *(LAS u32x4*)(L + L_Q + 16 * n), q1 = *(LAS u32x4*)(L + L_Q + 256 + 16 * n);
      constexpr float C2 = 0.0625f * 1.4426950408889634f;
      float m = -INFINITY;
      LAS float* sp = (LAS float*)(L + L_P) + 8 * ci + kg + 4 * (n & 1);
      auto score = [&](int t, const u32x4 (&k)[4]) {
        float s0 = 0.f, s1 = 0.f;
#pragma unroll
        for (int j = 0; j < 4; ++j) { s0 = dot2(k[0][j], q0[j], s0); s1 = dot2(k[2][j], q0[j], s1); }
#pragma unroll
        for (int j = 0; j < 4; ++j) { s0 = dot2(k[1][j], q1[j], s0); s1 = dot2(k[3][j], q1[j], s1); }
        s0 += dpp<XOR1>(s0); s1 += dpp<XOR1>(s1);
        float s = (n & 1) ? s1 : s0;
        s += dpp<XOR2>(s); s += dpp<ROR4>(s); s += dpp<ROR8>(s);
        s = rbf(s) * C2;
        m = fmaxf(m, s);
        if (n < 2) sp[t * 32] = s;
      };
      score(0, pre[0]);
#pragma unroll 1
      for (int t = PRE; t < NPH; ++t) { u32x4 k[4]; fetch(t, k); score(t, k); }
#pragma unroll
      for (int t = 1; t < PRE; ++t) score(t, pre[t]);
      m = fmaxf(m, dpp<XOR1>(m));
      m = x32_max(x16_max(m));
      if (lane == 0) ((LAS float*)(L + L_MW))[ci] = m;
      waitl();
      if (lane == 0) __hip_atomic_fetch_add((LAS unsigned*)(L + L_C1), 1u, RLX_WG);
      lds_wait<0>(L, L_C1, 4u);
      const f32x4 mw = *(LAS f32x4*)(L + L_MW);
      const float M = fmaxf(fmaxf(mw[0], mw[1]), fmaxf(mw[2], mw[3]));
      LAS unsigned char* pp = L + L_P + (ci * 64 + lane) * 32;
      const f32x4 e0 = *(LAS f32x4*)pp, e1 = *(LAS f32x4*)(pp + 16);
      u32x4 hi, lo2;
      float l = 0.f;
#pragma unroll
      for (int j = 0; j < 4; ++j) {
        const float a = __builtin_amdgcn_exp2f((j < 2 ? e0[2 * j] : e1[2 * j - 4]) - M), b = __builtin_amdgcn_exp2f((j < 2 ? e0[2 * j + 1] : e1[2 * j - 3]) - M);
        l += a + b;
        hi[j] = pk2(a, b);
        lo2[j] = pk2(a - bflo(hi[j]), b - bfhi(hi[j]));
      }
      *(LAS u32x4*)pp = hi; *(LAS u32x4*)(pp + 16) = lo2;
      l = wave_sum(l);
      if (lane == 0) ((LAS float*)(L + L_LW))[ci] = l;
      waitl();
      if (lane == 0) __hip_atomic_fetch_add((LAS unsigned*)(L + L_C2), 1u, RLX_WG);
      lds_wait<0>(L, L_C2, 4u);
    }
    {
      const int q = n >> 2;
      const int lo = q * 512 + (ci ^ (q & 1)) * 128 + kg * 32 + (n & 3) * 8;
      const bool act = q == kg && (n & 3) < 2;
      unsigned pa = act ? L_P + (n & 1) * 16 : L_ZERO;
      const unsigned pinc = act ? 128u : 0u;
      int off = (lane & 1) ? OOB : W_RECA + gl * RECA + (64 * ci + lane) * 2;
      pin(off);
      f32x4 acc = {0.f, 0.f, 0.f, 0.f};
      float sc = 0.f;
#pragma unroll 1
      for (int t = NPH; t < NSTEP; ++t) {
        u32x4 a[4];
#pragma unroll
        for (int m = 0; m < 4; ++m) a[m] = *(LAS u32x4*)(L + pa + 32 * m);
        pa += pinc;
        if (t == NSTEP - 4) { lds_wait<1>(L, L_SCRDY, 1u); sc = *(LAS float*)(L + L_SCALE); }
        if ((int)full <= t) full = lds_wait<VSLEEP>(L, L_FULL, t + 1);
        LAS unsigned char* sb = L + (t & (NSLOT - 1)) * SLOT_BYTES + lo;
        u32x2 v[8];
#pragma unroll
        for (int j = 0; j < 8; ++j) v[j] = tr_read(sb + 2048 * j);
#pragma unroll
        for (int m = 0; m < 4; ++m) acc = mfma(a[m], u32x4{v[2 * m][0], v[2 * m][1], v[2 * m + 1][0], v[2 * m + 1][1]}, acc);
        waitl();
        *vfree = t + 1;
      }
      if (wave == 1) ST(4);
      const float o = (acc[0] + acc[1]) * sc;
      __builtin_amdgcn_raw_buffer_store_b32(canonu(pk2(o, dpp<XOR1>(o))), rs, off, 0, AUX_SC1);
      if (lane == 0) __hip_atomic_fetch_add((LAS unsigned*)(L + L_PUB), 1u, RLX_WG);
      if (wave == 1) ST(5);
    }
    return;
  }
  const int tw = wave - 5;
  const int rb = role_b(g), jc = role_c(g);
  const int ks = rb >> 3, cs = rb & 7;
  auto invalidate = [&]() {
    const u32x4 sv = {SENT, SENT, SENT, SENT};
    __builtin_amdgcn_raw_buffer_store_b128(sv, rs, lane < 32 ? W_RECA + gl * RECA + 16 * lane : OOB, 0, AUX_SC1);
    if (rb >= 0) __builtin_amdgcn_raw_buffer_store_b128(sv, rs, lane < 32 ? W_RECB + ((ks * 2 + kg) * D + 64 * cs + 4 * n) * 4 : OOB, 0, AUX_SC1);
    if (jc >= 0) {
#pragma unroll
      for (int i = 0; i < 4; ++i) __builtin_amdgcn_raw_buffer_store_b128(sv, rs, lane < 56 ? W_RECC + jc * (2 * D * 4) + (56 * i + lane) * 16 : OOB, 0, AUX_SC1);
    }
  };
  if (EARLYINV && tw == 4) invalidate();
  if (EARLYINV == 2 && tw == 0 && (gi >> 3) == 2)
    __builtin_amdgcn_raw_buffer_store_b128(u32x4{0u, 0u, 0u, 0u}, rs, W_QG + ((lane >> 5) * HD + 64 * ((g & 7) * 8 + (gi & 7))) * 8 + (lane & 31) * 16, 0, AUX_SC1);
  u64 tag;
  {
    const bool qprod = (gi >> 3) == 2;
    const int qc = 64 * ((g & 7) * 8 + (gi & 7));
    unsigned z = 0;
    pin(z);
    const u64 gen = __hip_atomic_load((GAS u64*)(ws + W_GEN + z), RLX_AGENT);
    if (qprod) {
      u32x4 wqr[8], xa[2];
      const int row0 = 64 * tw + 8 * (lane >> 3);
      u32x4 xr[2];
#pragma unroll
      for (int b = 0; b < 2; ++b) { xa[b] = *(const GAS u32x4*)(x + b * D + 8 * min(lane, 55)); xr[b] = *(const GAS u32x4*)(x + b * D + row0); }
#pragma unroll
      for (int i = 0; i < 8; ++i) wqr[i] = __builtin_nontemporal_load((const GAS u32x4*)(wq + (size_t)(row0 + i) * HD + qc + 8 * (lane & 7)));
      __builtin_amdgcn_sched_barrier(0);
      if (QFIRST) { __builtin_amdgcn_s_barrier(); asm volatile("" ::: "memory"); }
      __builtin_amdgcn_sched_barrier(0);
      float a[2][8];
#pragma unroll
      for (int b = 0; b < 2; ++b) {
        if (lane >= 56) xa[b] = u32x4{0u, 0u, 0u, 0u};
        float xs[8]; float s = 0.f;
#pragma unroll
        for (int i = 0; i < 4; ++i) { xs[2 * i] = bflo(xa[b][i]); xs[2 * i + 1] = bfhi(xa[b][i]); s += xs[2 * i] + xs[2 * i + 1]; }
        const float mean = wave_sum(s) * (1.f / D);
        float qv = 0.f;
#pragma unroll
        for (int i = 0; i < 8; ++i) { const float d = xs[i] - mean; qv = fmaf(d, d, qv); }
        if (lane >= 56) qv = 0.f;
        const float var = wave_sum(qv) * (1.f / D);
        const float mb = rbf(mean);
        const float rsd = __builtin_amdgcn_rcpf(rbf(__builtin_amdgcn_sqrtf(rbf(rbf(var) + 1e-5f))));
#pragma unroll
        for (int i = 0; i < 4; ++i) {
          const unsigned d2 = pk2(bflo(xr[b][i]) - mb, bfhi(xr[b][i]) - mb);
          xr[b][i] = pk2(bflo(d2) * rsd, bfhi(d2) * rsd);
        }
#pragma unroll
        for (int j = 0; j < 8; ++j) a[b][j] = 0.f;
      }
      u32x4 pr[8];
      pairrows(wqr, pr);
#pragma unroll
      for (int b = 0; b < 2; ++b)
#pragma unroll
        for (int j = 0; j < 8; ++j)
#pragma unroll
          for (int i = 0; i < 4; ++i) a[b][j] = dot2(pr[j][i], xr[b][i], a[b][j]);
      float b8[8], b4[4];
#pragma unroll
      for (int j = 0; j < 8; ++j) b8[j] = tr32(a[0][j], a[1][j]);
#pragma unroll
      for (int j = 0; j < 4; ++j) { b4[j] = tr16(b8[j], b8[j + 4]); b4[j] += dpp<ROR8>(b4[j]); }
      if ((lane & 8) == 0) *LP(f32x4, L_QPART + (tw * 128 + (lane >> 5) * 64 + 8 * (lane & 7) + 4 * ((lane >> 4) & 1)) * 4) = f32x4{b4[0], b4[1], b4[2], b4[3]};
      waitl();
      if (lane == 0) __hip_atomic_fetch_add((LAS unsigned*)(L + L_QCNT), 1u, RLX_WG);
    } else if (QFIRST) {
      __builtin_amdgcn_s_barrier(); asm volatile("" ::: "memory");
    }
    tag = mix64(((u64)__builtin_amdgcn_readfirstlane((unsigned)(gen >> 32)) << 32) | __builtin_amdgcn_readfirstlane((unsigned)gen));
    if (tw == 0) {
      const u64 t48 = tag & ~0xFFFFull;
      if (qprod) {
        lds_wait<0>(L, L_QCNT, 7u);
        ST(1);
#pragma unroll
        for (int b = 0; b < 2; ++b) {
          float qs = 0.f;
#pragma unroll
          for (int w = 0; w < 7; ++w) qs += *LP(float, L_QPART + (w * 128 + b * 64 + lane) * 4);
          __hip_atomic_store((GAS u64*)(ws + W_QG) + b * HD + qc + lane, t48 | f2bf(qs), RLX_AGENT);
        }
      }
      lds_wait<2>(L, L_FULL, qprod ? 1 : QPOLL);
      GAS u64* qg = (GAS u64*)(ws + W_QG) + bh * DH + lane;
      u64 v[4];
#pragma unroll 1
      for (unsigned spins = 0; spins < SPIN_CAP; ++spins) {
        bool ok = true;
#pragma unroll
        for (int i = 0; i < 4; ++i) v[i] = __hip_atomic_load(qg + 64 * i, RLX_AGENT);
#pragma unroll
        for (int i = 0; i < 4; ++i) ok &= (v[i] & ~0xFFFFull) == t48;
        if (__all(ok)) break;
        if (QSLEEP) __builtin_amdgcn_s_sleep(QSLEEP);
      }
#pragma unroll
      for (int i = 0; i < 4; ++i) ((LAS unsigned short*)(L + L_Q))[64 * i + lane] = (unsigned short)v[i];
      waitl();
      *(volatile LAS unsigned*)(L + L_QRDY) = 1u;
      ST(2);
    }
  }
  auto exchange = [&]() {
    if (!EARLYINV && INVAT) { lds_wait<16>(L, L_FULL, INVAT + gi); invalidate(); }
    lds_wait<8>(L, L_C2, 4u);
    if (XSTAG) lds_wait<16>(L, L_FULL, NPH + 2 + gi * XSTAG / 32);
    if (!EARLYINV && !INVAT) invalidate();
    const f32x4 mw = *(LAS f32x4*)(L + L_MW), lw = *(LAS f32x4*)(L + L_LW);
    const float Mw = fmaxf(fmaxf(mw[0], mw[1]), fmaxf(mw[2], mw[3])), Lw = (lw[0] + lw[1]) + (lw[2] + lw[3]);
    __builtin_amdgcn_raw_buffer_store_b64(u32x2{__float_as_uint(Mw), __float_as_uint(Lw)}, rs, lane == 0 ? W_ML + gl * 8 : OOB, 0, AUX_SC1);
    waitv<0>();
    if (lane == 0) __hip_atomic_store((GAS u64*)(ws + W_FLAG) + gl, tag, RLX_AGENT);
    lds_wait<16>(L, L_FULL, ML_STEP + gi * RSTAG / 32);
    const bool all = rb >= 0 || jc >= 0;
    GAS u64* fp = (GAS u64*)(ws + W_FLAG) + (all ? lane : 8 * bh + (lane & 7));
    const int fs = all ? 64 : 0;
#pragma unroll 1
    for (unsigned spins = 0; spins < SPIN_CAP; ++spins) {
      bool ok = true;
#pragma unroll
      for (int i = 0; i < 4; ++i) ok &= __hip_atomic_load(fp + fs * i, RLX_AGENT) == tag;
      if (__all(ok)) break;
      __builtin_amdgcn_s_sleep(16);
    }
    asm volatile("" ::: "memory");
    const u64 ml = __hip_atomic_load((GAS u64*)(ws + W_ML) + 8 * bh + (lane & 7), RLX_AGENT);
    const float mi = __uint_as_float((unsigned)ml), li = __uint_as_float((unsigned)(ml >> 32));
    float Mg = fmaxf(mi, dpp<XOR1>(mi)); Mg = fmaxf(Mg, dpp<XOR2>(Mg)); Mg = fmaxf(Mg, dpp<HMIR>(Mg));
    float Lg = li * __builtin_amdgcn_exp2f(mi - Mg);
    Lg += dpp<XOR1>(Lg); Lg += dpp<XOR2>(Lg); Lg += dpp<HMIR>(Lg);
    *(volatile LAS float*)(L + L_SCALE) = __builtin_amdgcn_exp2f(Mw - Mg) / Lg;
    waitl();
    *(volatile LAS unsigned*)(L + L_SCRDY) = 1u;
    if (g == 0 && lane == 0) __hip_atomic_store((GAS u64*)(ws + W_GEN), tag, RLX_AGENT);
  };
  if (TLBPF && tw == 6) {
    const char* kb = (const char*)kc + chunk;
    const char* vb = (const char*)vc + chunk;
#pragma unroll 1
    for (int i = 1; i < 32; ++i) {
      if (TLBPF == 1) lds_wait<8>(L, L_FULL, max(4 * i - TLBAHEAD, 1));
      const char* a = (i < 16 ? kb : vb) + (i & 15) * 65536;
      unsigned t;
      asm volatile("s_load_dword %0, %1, 0x0\n\ts_waitcnt lgkmcnt(0)" : "=s"(t) : "s"(a) : "memory");
    }
  }
  if (tw == 5) {
    lds_wait<2>(L, L_FULL, LEADAT);
    *(volatile LAS unsigned*)(L + L_CORR) = *(volatile LAS unsigned*)(L + L_E) + ((gi >> 3) == 2 ? -QLAG : LEAD) * 128;
  }
  auto pace = [&]() {
    const unsigned tag32 = (unsigned)(tag >> 32);
    unsigned A = __builtin_amdgcn_readfirstlane(*(volatile LAS unsigned*)(L + L_CORR)), prev = 0;
    int Bd = 0, tk = 0, f = 0;
    const int adv = rb >= 0 ? ADVB : jc >= 0 ? ADVC : 0;
    auto run_until = [&](int tt) {
#pragma unroll 1
      while (f < tt) {
        const int nf = (int)lds_wait<8>(L, L_FULL, f + 1);
        f = max(nf, f + 1);
        *(volatile LAS unsigned*)(L + L_CORR) = A + Bd * (f - tk) - adv * f;
      }
    };
#pragma unroll 1
    for (int k = 0; k < NCP; ++k) {
      const int tc = CP0 + CPS * k;
      run_until(tc);
      const unsigned e = __builtin_amdgcn_readfirstlane(*(volatile LAS unsigned*)(L + L_E));
      if (lane == 0 && (g >> 5) == 7) __hip_atomic_store((GAS u64*)(ws + W_PROG) + k * 256 + (g & 31), ((u64)tag32 << 32) | e, RLX_AGENT);
      run_until(tc + CPD + (STAG ? (g >> 3) % STAG : 0));
      float sum = 0.f, cnt = 0.f;
      {
        const u64 pv = __hip_atomic_load((GAS u64*)(ws + W_PROG) + k * 256 + (lane & 31), RLX_AGENT);
        const bool ok = (unsigned)(pv >> 32) == tag32;
        sum += ok ? (float)(int)((unsigned)pv - e) : 0.f; cnt += ok ? 1.f : 0.f;
      }
      sum = wave_sum(sum); cnt = wave_sum(cnt);
      const unsigned eb = e + __builtin_amdgcn_readfirstlane((int)(sum / fmaxf(cnt, 1.f)));
      if (k) Bd = max(BLO, min(BHI, (int)(eb - prev) / CPS - BIAS));
      prev = eb; A = eb; tk = tc;
    }
    run_until(NSTEP - 6);
    *(volatile LAS unsigned*)(L + L_TFIN) = A + Bd * (NSTEP - tk) + NSTEP * TSCHED;
  };
  if (jc < 0) {
    if (tw == 5) pace();
    if (tw == 4) exchange();
  }
  auto wait_weights = [&](int step) {
    if (step > NSTEP) { lds_wait<8>(L, L_FULL, NSTEP - 2); lds_wait<1>(L, L_PUB, 4u); } else lds_wait<TWSLEEP>(L, L_FULL, step);
  };
  if (jc < 0) {
    if (rb < 0) {
      return;
    }
    if (tw >= 4) return;
    wait_weights(TWB + TWS * tw);
    u32x4 fr[2][8];
    {
      const __amdgpu_buffer_rsrc_t r = __builtin_amdgcn_make_buffer_rsrc((void*)wo, 0, HD * D * 2, 0x00020000);
      u32x4 raw[2][8];
      const int voff = tile_voff<D, 32, 8>(lane);
#pragma unroll
      for (int s = 0; s < 2; ++s) ldtile<D>(raw[s], r, (512 * ks + 128 * tw + 64 * s) * D + 64 * cs, voff);
#pragma unroll
      for (int s = 0; s < 2; ++s) mkfrag(raw[s], fr[s]);
    }
    unsigned off = W_RECA + (((kg & 1) * 16 + 2 * ks + (tw >> 1)) * 8 + (kg >> 1)) * RECA + 256 * (tw & 1) + 16 * n;
    unsigned ta = tw * 4352 + kg * 272 + 16 * n, tb = tw * 4352 + n * 272 + 16 * kg;
    unsigned wa = L_RED + ((tw * 2 + (lane >> 5)) * 64 + ccol(n) + 2 * (kg & 1)) * 4, ra = L_RED + (lane >> 5) * 1024 + (tw * 32 + (lane & 31)) * 4;
    int so = lane < 32 ? W_RECB + ((ks * 2 + (tw >> 1)) * D + 64 * cs + 32 * (tw & 1) + lane) * 4 : OOB;
    pin(ta); pin(tb);
    u32x4 v[4];
#pragma unroll
    for (int i = 0; i < 4; ++i) { v[i] = u32x4{SENT, SENT, SENT, SENT}; pin(v[i]); }
    pin(off); pin(wa); pin(ra); pin(so);
    lds_wait<4>(L, L_SCRDY, 1u);
    if (WARMC && tw == 3) { lds_wait<8>(L, L_FULL, WARMC); warm_code(lane); }
    lds_wait<1>(L, L_PUB, 4u);
    if (BX) {
      const unsigned tf = __builtin_amdgcn_readfirstlane(*(volatile LAS unsigned*)(L + L_TFIN)) + BX * 128;
#pragma unroll 1
      for (int i = 0; i < 64 && (int)(((unsigned)__builtin_amdgcn_s_memrealtime() << 7) - tf) < 0; ++i) __builtin_amdgcn_s_sleep(1);
    }
    gather4<1, SB>(v, off, ws, 2 * RECA);
    if (tw == 0) STY(6);
#pragma unroll
    for (int i = 0; i < 4; ++i) *LP(u32x4, ta + i * 1088) = v[i];
    waitl();
#pragma unroll
    for (int i = 0; i < 4; ++i) v[i] = *LP(u32x4, tb + i * 64);
    f32x4 d[4];
#pragma unroll
    for (int c = 0; c < 4; ++c) d[c] = mfma(v[0], fr[0][c], f32x4{0.f, 0.f, 0.f, 0.f});
#pragma unroll
    for (int s = 1; s < 4; ++s)
#pragma unroll
      for (int c = 0; c < 4; ++c) d[c] = mfma(v[s], fr[s >> 1][4 * (s & 1) + c], d[c]);
    float e[4];
#pragma unroll
    for (int c = 0; c < 4; ++c) e[c] = tr32(d[c][0] + d[c][2], d[c][1] + d[c][3]);
    *LP(f32x2, wa) = f32x2{tr16(e[0], e[2]), tr16(e[1], e[3])};
    bar();
    {
      const float s = x32_sum(*LP(float, ra) + *LP(float, ra + 512));
      __builtin_amdgcn_raw_buffer_store_b32(canon(s), rs, so, 0, AUX_SC1);
      if (tw == 0) STY(7);
    }
    return;
  }
#define TS(i) do {} while (0)
  u32x4 fg[2][8], fd[8];
  {
    const GAS char* gp = (const GAS char*)((n & 8) ? wu : wg) + ((64 * tw + 16 * kg) * FF + 64 * jc + 8 * (n & 7)) * 2;
    const __amdgpu_buffer_rsrc_t r3 = __builtin_amdgcn_make_buffer_rsrc((void*)wd, 0, FF * D * 2, 0x00020000);
    wait_weights(tw == 5 ? 6 : TWC + TWS * tw);
    u32x4 raw[2][8];
#pragma unroll
    for (int j = 0; j < 16; ++j) raw[j >> 3][j & 7] = __builtin_nontemporal_load((const GAS u32x4*)(gp + j * (FF * 2)));
    __builtin_amdgcn_sched_barrier(0);
    pairrows(raw[0], fg[0]); pin8(fg[0]);
    pairrows(raw[1], fg[1]); pin8(fg[1]);
    if (!WDLATE) { ldtile<D>(raw[0], r3, 64 * jc * D + 64 * tw, tile_voff<D, 32, 8>(lane)); pairrows(raw[0], fd); pin8(fd); }
    if (tw == 5) pace();
    if (tw == 4) exchange();
    if (WARMC && tw == 3) { lds_wait<8>(L, L_FULL, WARMC); warm_code(lane); warm_code(lane + 64); }
  }
  const int b = (n >> 2) & 1;
  const int col = 64 * tw + 16 * kg + 4 * (n & 3);
  const u32x2 xp = *(const GAS u32x2*)(x + b * D + col);
  f32x4 xf = {bflo(xp[0]), bfhi(xp[0]), bflo(xp[1]), bfhi(xp[1])};
  unsigned off = W_RECB + (((n & 8) + b) * D + col) * 4;
  unsigned a_h = L_H + (b * D + col) * 2, a_st = L_ST + (b * 8 + tw) * 8 + (lane >> 5) * 4;
  unsigned a_red = L_RED + tw * 1536 + (n >> 2) * 512 + (kg >> 1) * 256 + (kg & 1) * 16;
  unsigned a_rd = L_RED + tw * 512 + lane * 4, a_rc = L_RED + 1024 + lane * 4, a_sr = L_ST + (tw * 8 + (lane & 7)) * 8;
  unsigned a_g = L_G + (tw * 64 + 8 * (lane & 7) + (lane >> 3)) * 2, a_gr = L_G + (b * 64 + 32 * (n & 1) + 8 * kg) * 2;
  int so = lane < 32 ? W_RECC + ((jc * 2 + kg) * D + 64 * tw + ccol(n)) * 4 : OOB;
  if (!((0x111 >> n) & 1)) { a_red = tw * 2048 + lane * 16; a_st = 16384 + tw * 256 + lane * 4; }
  unsigned msel = (lane & 8) ? ~0u : 0u;
  u64 wr = __ballot((0x111 >> n) & 1);
  const int pg = lane >> 3, ch = lane & 7;
  unsigned offd = W_RECC + pg * (2 * D * 4) + (32 * jc + 4 * ch) * 4, a_hd = L_H + (32 * jc + 4 * ch + 2 * (lane >> 5)) * 2;
  int oo = (lane & 24) ? OOB : (32 * jc + 4 * ch + 2 * (lane >> 5)) * 2;
  u32x4 v[4], w[4], b0, b1;
  constexpr unsigned ONE2 = 0x3F803F80u;
  b0 = u32x4{ONE2, ONE2, ONE2, ONE2}; b1 = b0;
#pragma unroll
  for (int i = 0; i < 4; ++i) {
    v[i] = u32x4{SENT, SENT, SENT, SENT}; pin(v[i]);
  }
  pin(xf); pin(off); pin(a_h); pin(a_st); pin(a_red); pin(a_rd); pin(a_sr); pin(a_g); pin(a_gr); pin(so); pin(offd); pin(a_hd); pin(oo);
  pin(a_rc); pin(msel); pin(b0); pin(b1);
  const __amdgpu_buffer_rsrc_t ro = __builtin_amdgcn_make_buffer_rsrc((void*)out, 0, 2 * D * 2, 0x00020000);
  lds_wait<4>(L, L_SCRDY, 1u);
  if (WDLATE) {
    const __amdgpu_buffer_rsrc_t r3 = __builtin_amdgcn_make_buffer_rsrc((void*)wd, 0, FF * D * 2, 0x00020000);
    int vd = tile_voff<D, 32, 8>(lane);
    pin(vd);
    wait_weights(NSTEP + 1);
    u32x4 raw[8];
    ldtile<D>(raw, r3, 64 * jc * D + 64 * tw, vd);
    pairrows(raw, fd); pin8(fd);
  } else {
    lds_wait<1>(L, L_PUB, 4u);
  }
  if (tw == 0) ST(15);
  if (CX) {
    const unsigned tf = __builtin_amdgcn_readfirstlane(*(volatile LAS unsigned*)(L + L_TFIN)) + CX * 128;
#pragma unroll 1
    for (int i = 0; i < 64 && (int)(((unsigned)__builtin_amdgcn_s_memrealtime() << 7) - tf) < 0; ++i) __builtin_amdgcn_s_sleep(1);
  }
  gather4<RC, SC>(v, off, ws, 2 * D * 4);
  TS(5);
  if (tw == 0) STY(8);
  {
    f32x4 a = (__builtin_bit_cast(f32x4, v[0]) + __builtin_bit_cast(f32x4, v[1])) + (__builtin_bit_cast(f32x4, v[2]) + __builtin_bit_cast(f32x4, v[3]));
#pragma unroll
    for (int j = 0; j < 4; ++j) { float t = a[j] + dpp<ROR8>(a[j]); pin(t); a[j] = t; }
    const unsigned a01 = pk2(a[0], a[1]), a23 = pk2(a[2], a[3]);
    const unsigned h0 = pk2(xf[0] + bflo(a01), xf[1] + bfhi(a01)), h1 = pk2(xf[2] + bflo(a23), xf[3] + bfhi(a23));
#define ACT(o, v, ctrl) o = (unsigned)__builtin_amdgcn_update_dpp((int)(o), (int)(v), ctrl, 0xf, 0x3, false)
    ACT(b0[0], h0, 0x00); ACT(b0[1], h1, 0x00); ACT(b0[2], h0, 0x55); ACT(b0[3], h1, 0x55);
    ACT(b1[0], h0, 0xAA); ACT(b1[1], h1, 0xAA); ACT(b1[2], h0, 0xFF); ACT(b1[3], h1, 0xFF);
    f32x4 d[8];
#pragma unroll
    for (int c = 0; c < 8; ++c) {
      d[c] = mfma(fg[1][c], b1, mfma(fg[0][c], b0, f32x4{0.f, 0.f, 0.f, 0.f}));
      if (c) *LP(f32x4, a_red + (c - 1) * 32) = d[c - 1];
      __builtin_amdgcn_sched_group_barrier(0x8, 2, 0); __builtin_amdgcn_sched_group_barrier(0x2, 3, 0); __builtin_amdgcn_sched_group_barrier(0x200, 1, 0);
    }
    *LP(f32x4, a_red + 7 * 32) = d[7];
    float s1 = dot2(h1, ONE2, dot2(h0, ONE2, 0.f)), s2 = dot2(h1, h1, dot2(h0, h0, 0.f));
    s1 = x16_sum(tr32(s1, s2));
    s1 += dpp<XOR1>(s1); s1 += dpp<XOR2>(s1);
    *LP(u32x2, a_h) = u32x2{h0, h1};
    *LP(float, a_st) = s1;
  }
  TS(6);
  bar();
  TS(7);
  if (tw == 0) STY(12);
  if (tw < 2) {
    f32x2 st = *LP(f32x2, a_sr);
    f32x2 ps[7], pc[7];
#pragma unroll
    for (int i = 0; i < 7; ++i) {
      ps[i] = f32x2{*LP(float, a_rd + i * 1536), *LP(float, a_rd + i * 1536 + 256)};
      pc[i] = f32x2{*LP(float, a_rc + i * 1536), *LP(float, a_rc + i * 1536 + 256)};
    }
    __builtin_amdgcn_sched_barrier(0);
#pragma unroll
    for (int j = 0; j < 2; ++j) { st[j] += dpp<XOR1>(st[j]); st[j] += dpp<XOR2>(st[j]); st[j] += dpp<HMIR>(st[j]); }
    const float mean = st[0] * (1.f / D);
    const float var = fmaxf(fmaf(-mean, mean, st[1] * (1.f / D)), 0.f);
    const float mb = rbf(mean);
    const float rsd = __builtin_amdgcn_rcpf(rbf(__builtin_amdgcn_sqrtf(rbf(rbf(var) + 1e-5f))));
    const f32x2 sm = ((ps[0] + ps[1]) + (ps[2] + ps[3])) + ((ps[4] + ps[5]) + ps[6]);
    const f32x2 cm = ((pc[0] + pc[1]) + (pc[2] + pc[3])) + ((pc[4] + pc[5]) + pc[6]);
    const unsigned gu = pk2((sm[0] - mb * cm[0]) * rsd, (sm[1] - mb * cm[1]) * rsd);
    const float xg = bflo(gu);
    constexpr float K1 = -2.f * 0.7978845608028654f * 1.4426950408889634f;
    const float ge = xg * __builtin_amdgcn_rcpf(1.f + __builtin_amdgcn_exp2f(xg * fmaf(xg * xg, K1 * 0.044715f, K1)));
    *LP(unsigned short, a_g) = (unsigned short)f2bf(rbf(ge) * bfhi(gu));
  }
  drain4<RC>(v);
#pragma unroll
  for (int i = 0; i < 4; ++i) {
    const unsigned iv = (i < 3 || pg < 4) ? SENT : 0u;
    if (tw == 6) { w[i] = u32x4{iv, iv, iv, iv}; pin(w[i]); }
  }
  bar();
  if (tw == 0) STY(13);
  {
    const u32x4 a0 = *LP(u32x4, a_gr);
    f32x4 d[8];
#pragma unroll
    for (int c = 0; c < 8; ++c) d[c] = mfma(a0, fd[c], f32x4{0.f, 0.f, 0.f, 0.f});
    u32x4 o;
#pragma unroll
    for (int c = 0; c < 4; ++c) {
      const unsigned keep = (__float_as_uint(d[c + 4][1]) & msel) | (__float_as_uint(d[c][0]) & ~msel);
      const unsigned send = (__float_as_uint(d[c][1]) & msel) | (__float_as_uint(d[c + 4][0]) & ~msel);
      o[c] = canon(__uint_as_float(keep) + dpp<ROR8>(__uint_as_float(send)));
    }
    __builtin_amdgcn_raw_buffer_store_b128(o, rs, so, 0, AUX_SC1);
    if (tw == 0) ST(9);
  }
  if (tw == 6) {
    const unsigned hv = *LP(unsigned, a_hd);
    if (DDELAY) __builtin_amdgcn_s_sleep(DDELAY);
    gather4<RD, SD>(w, offd, ws, 8 * (2 * D * 4));
    ST(10);
    const f32x4 t = (__builtin_bit_cast(f32x4, w[0]) + __builtin_bit_cast(f32x4, w[1])) + (__builtin_bit_cast(f32x4, w[2]) + __builtin_bit_cast(f32x4, w[3]));
    float s0 = tr32(t[0], t[2]), s1 = tr32(t[1], t[3]);
    s0 = x16_sum(s0); s1 = x16_sum(s1);
    s0 += dpp<ROR8>(s0); s1 += dpp<ROR8>(s1);
    const unsigned dn = pk2(s0, s1);
    __builtin_amdgcn_raw_buffer_store_b32(pk2(bflo(hv) + bflo(dn), bfhi(hv) + bfhi(dn)), ro, oo, 0, 0);
    drain4<RD>(w);
    ST(11);
  }
}

extern "C" void kernel_launch(void* const* d_in, const int* in_sizes, int n_in, void* d_out, int out_size, void* d_ws,
                              size_t ws_size, hipStream_t stream) {
  (void)hipFuncSetAttribute((const void*)fused_block, hipFuncAttributeMaxDynamicSharedMemorySize, LDS_BYTES);
  hipLaunchKernelGGL(fused_block, dim3(NWG), dim3(NTHR), LDS_BYTES, stream, (const bf16_t*)d_in[0], (const bf16_t*)d_in[1],
                     (const bf16_t*)d_in[2], (const bf16_t*)d_in[3], (const bf16_t*)d_in[4], (const bf16_t*)d_in[6],
                     (const bf16_t*)d_in[7], (const bf16_t*)d_in[8], (bf16_t*)d_out, (unsigned char*)d_ws + (WSEND ? ((ws_size - (8u << 20)) & ~(size_t)4095) : 0));
}
```

```cpp
#include <hip/hip_runtime.h>
#include <hip/hip_bf16.h>
#include <stdint.h>

typedef unsigned short bf16_t;
typedef unsigned long long u64;
typedef unsigned u32x4 __attribute__((ext_vector_type(4)));
typedef unsigned u32x2 __attribute__((ext_vector_type(2)));
typedef float f32x4 __attribute__((ext_vector_type(4)));
typedef float f32x2 __attribute__((ext_vector_type(2)));
typedef short s16x4 __attribute__((ext_vector_type(4)));
typedef __bf16 bf16x2 __attribute__((ext_vector_type(2)));
typedef __bf16 bf16x8 __attribute__((ext_vector_type(8)));
#define LAS __attribute__((address_space(3)))
#define GAS __attribute__((address_space(1)))
#define LP(T, a) ((LAS T*)(size_t)(a))
#define LV(T, a) (*(volatile LAS T*)(size_t)(a))
#define RLX_AGENT __ATOMIC_RELAXED, __HIP_MEMORY_SCOPE_AGENT
#define RLX_WG __ATOMIC_RELAXED, __HIP_MEMORY_SCOPE_WORKGROUP
#define STR2(x) #x
#define STR(x) STR2(x)

constexpr int D = 448, DH = 256, HD = 4096, FF = 1792;
constexpr int NWG = 256, NTHR = 768;
constexpr int NPH = 64, NSTEP = 2 * NPH;
constexpr int NSLOT = 8, SLOT_BYTES = 16384;
#define W0 18
#define WMIN 15
#define WMAX 32
#define GAIN 10
#define TSCHED 6940
constexpr int WSTART = 22;
constexpr int PRE = 9;
constexpr int NCP = 5, CP0 = 14, CPS = 25, CPD = 4;
constexpr int BIAS = 60, BHI = 200;
constexpr int QLAG = 150, ADVC = 40;
constexpr int QPOLL = 5, TW = 20, ML_STEP = 96;
constexpr int BX = 125, CX = 228, DDELAY = 8;
static_assert(WMIN >= 13 && WMIN <= W0 && W0 <= WMAX && WMIN <= WSTART && WSTART <= WMAX && WMAX <= 44, "window");
constexpr int L_CTL = NSLOT * SLOT_BYTES;
constexpr int L_FREE = L_CTL, L_QCNT = L_CTL + 20, L_QRDY = L_CTL + 24, L_C1 = L_CTL + 28, L_C2 = L_CTL + 32, L_SCRDY = L_CTL + 36,
              L_SCALE = L_CTL + 40, L_PUB = L_CTL + 44, L_CORR = L_CTL + 48, L_TFIN = L_CTL + 52, L_MW = L_CTL + 64, L_LW = L_CTL + 80;
constexpr int L_FULL = L_CTL + 128;
constexpr int L_E = L_FULL + 256;
constexpr int L_Q = L_E + 256;
constexpr int L_P = L_Q + 512;
constexpr int L_ZERO = L_P + 8192;
constexpr int L_ST = L_ZERO + 128;
constexpr int L_RED = L_ST + 128;
constexpr int L_H = L_RED + 10752;
constexpr int L_G = L_H + 1792;
constexpr int LDS_BYTES = L_G + 256;
static_assert(LDS_BYTES > 80 * 1024 && LDS_BYTES <= 160 * 1024, "one workgroup per CU");
constexpr int W_GEN = 0;
constexpr int W_FLAG = 4096;
constexpr int W_PROG = 8192;
constexpr int W_QG = 12288;
constexpr int W_RECA = W_QG + 65536, RECA = 512;
constexpr int W_RECB = W_RECA + 256 * RECA;
constexpr int W_RECC = W_RECB + 8 * 2 * D * 4;
constexpr int W_END = W_RECC + 28 * 2 * D * 4;
static_assert(W_END - W_RECB <= 256 * 512, "every workgroup invalidates 512 B of the B / C records");
constexpr unsigned SENT = 0xFFFFFFFFu;
constexpr int OOB = 0x40000000;
constexpr int AUX_SC1 = 16, AUX_NT = 2;
constexpr unsigned SPIN_CAP = 1u << 20;

__device__ __forceinline__ float bflo(unsigned v) { return __builtin_bit_cast(float, v << 16); }
__device__ __forceinline__ float bfhi(unsigned v) { return __builtin_bit_cast(float, v & 0xffff0000u); }
__device__ __forceinline__ unsigned f2bf(float f) { return __builtin_bit_cast(unsigned short, (__bf16)f); }
__device__ __forceinline__ float rbf(float f) { return bflo(f2bf(f)); }
__device__ __forceinline__ unsigned pk2(float lo, float hi) {
  bf16x2 v = {(__bf16)lo, (__bf16)hi};
  return __builtin_bit_cast(unsigned, v);
}
__device__ __forceinline__ float dot2(unsigned a, unsigned b, float acc) {
  return __builtin_amdgcn_fdot2_f32_bf16(__builtin_bit_cast(bf16x2, a), __builtin_bit_cast(bf16x2, b), acc, false);
}
template <int CTRL> __device__ __forceinline__ float dpp(float x) {
  return __builtin_bit_cast(float, __builtin_amdgcn_mov_dpp(__builtin_bit_cast(int, x), CTRL, 0xf, 0xf, true));
}
constexpr int XOR1 = 0xB1, XOR2 = 0x4E, HMIR = 0x141, MIR = 0x140, ROR4 = 0x124, ROR8 = 0x128;
__device__ __forceinline__ float row16_sum(float x) {
  x += dpp<XOR1>(x); x += dpp<XOR2>(x); x += dpp<HMIR>(x); x += dpp<MIR>(x);
  return x;
}
__device__ __forceinline__ float tr32(float a, float b) {
  auto s = __builtin_amdgcn_permlane32_swap(__float_as_uint(a), __float_as_uint(b), false, false);
  return __uint_as_float(s[0]) + __uint_as_float(s[1]);
}
__device__ __forceinline__ float tr16(float a, float b) {
  auto s = __builtin_amdgcn_permlane16_swap(__float_as_uint(a), __float_as_uint(b), false, false);
  return __uint_as_float(s[0]) + __uint_as_float(s[1]);
}
__device__ __forceinline__ float x16_sum(float x) { return tr16(x, x); }
__device__ __forceinline__ float x32_sum(float x) { return tr32(x, x); }
__device__ __forceinline__ float x16_max(float x) {
  auto s = __builtin_amdgcn_permlane16_swap(__float_as_uint(x), __float_as_uint(x), false, false);
  return fmaxf(__uint_as_float(s[0]), __uint_as_float(s[1]));
}
__device__ __forceinline__ float x32_max(float x) {
  auto s = __builtin_amdgcn_permlane32_swap(__float_as_uint(x), __float_as_uint(x), false, false);
  return fmaxf(__uint_as_float(s[0]), __uint_as_float(s[1]));
}
__device__ __forceinline__ float wave_sum(float x) { return x32_sum(x16_sum(row16_sum(x))); }
__device__ __forceinline__ float max4(f32x4 v) { return fmaxf(fmaxf(v[0], v[1]), fmaxf(v[2], v[3])); }
__device__ __forceinline__ float ln_rstd(float var) { return __builtin_amdgcn_rcpf(rbf(__builtin_amdgcn_sqrtf(rbf(rbf(var) + 1e-5f)))); }
__device__ __forceinline__ unsigned canonu(unsigned u) { return min(u, SENT - 1u); }
__device__ __forceinline__ unsigned canon(float f) { return canonu(__float_as_uint(f)); }
__device__ __forceinline__ f32x4 mfma(u32x4 a, u32x4 b, f32x4 c) {
  return __builtin_amdgcn_mfma_f32_16x16x32_bf16(__builtin_bit_cast(bf16x8, a), __builtin_bit_cast(bf16x8, b), c, 0, 0, 0);
}
__device__ __forceinline__ u32x2 tr_read(unsigned a) { return __builtin_bit_cast(u32x2, __builtin_amdgcn_ds_read_tr16_b64_v4i16(LP(s16x4, a))); }
__device__ __forceinline__ unsigned uni(unsigned v) { return (unsigned)__builtin_amdgcn_readfirstlane((int)v); }
__device__ __forceinline__ unsigned now128() { return (unsigned)__builtin_amdgcn_s_memrealtime() << 7; }
template <typename T> __device__ __forceinline__ void pin(T& v) { asm volatile("" : "+v"(v)); }
template <int N> __device__ __forceinline__ void waitv() { asm volatile("s_waitcnt vmcnt(%0)" ::"i"(N) : "memory"); }
__device__ __forceinline__ void waitl() { asm volatile("s_waitcnt lgkmcnt(0)" ::: "memory"); }
__device__ __forceinline__ void bar0() { __builtin_amdgcn_s_barrier(); asm volatile("" ::: "memory"); }
__device__ __forceinline__ void bar() { waitl(); bar0(); }
__device__ __forceinline__ void lds_arrive(int off, int lane) {
  waitl();
  if (lane == 0) __hip_atomic_fetch_add(LP(unsigned, off), 1u, RLX_WG);
}
template <int SLEEP> __device__ __forceinline__ unsigned lds_wait(int off, unsigned want) {
  unsigned v = 0;
#pragma unroll 1
  for (unsigned s = 0; s < SPIN_CAP; ++s) {
    v = uni(LV(unsigned, off));
    if (v >= want) break;
    if (SLEEP) __builtin_amdgcn_s_sleep(SLEEP);
  }
  asm volatile("" ::: "memory");
  return v;
}
__device__ __forceinline__ void lds_spin(unsigned addr, unsigned want) {
  unsigned v, cap = SPIN_CAP;
  asm volatile("1:\n\t"
               "ds_read_b32 %0, %2\n\t"
               "s_sub_u32 %1, %1, 1\n\t"
               "s_cbranch_scc1 2f\n\t"
               "s_waitcnt lgkmcnt(0)\n\t"
               "v_cmp_gt_u32 vcc, %3, %0\n\t"
               "s_cbranch_vccnz 1b\n"
               "2:\n\t"
               "s_waitcnt lgkmcnt(0)"
               : "=&v"(v), "+s"(cap) : "v"(addr), "s"(want) : "memory", "vcc", "scc");
}
__device__ __forceinline__ void wait_expected(int ticks) {
  const unsigned tf = uni(LV(unsigned, L_TFIN)) + ticks * 128;
#pragma unroll 1
  for (int i = 0; i < 64 && (int)(now128() - tf) < 0; ++i) __builtin_amdgcn_s_sleep(1);
}

#define DMA4(a) \
  "global_load_lds_dwordx4 " a ", s[60:61] offset:0 nt\n\t" \
  "global_load_lds_dwordx4 " a ", s[60:61] offset:1024 nt\n\t" \
  "global_load_lds_dwordx4 " a ", s[60:61] offset:2048 nt\n\t" \
  "global_load_lds_dwordx4 " a ", s[60:61] offset:3072 nt\n\t"
#define STEPADDR \
  "s_and_b32 s66, %[t], 1\n\t" \
  "s_lshl_b32 s66, s66, 13\n\t" \
  "s_bfe_u32 s67, %[t], 0x40001\n\t" \
  "s_lshl_b32 s67, s67, 16\n\t" \
  "s_or_b32 s66, s66, s67\n\t" \
  "s_bfe_u32 s67, %[t], 0x10005\n\t" \
  "s_lshl_b32 s67, s67, 12\n\t" \
  "s_or_b32 s66, s66, s67\n\t" \
  "s_add_u32 s60, s72, s66\n\t" \
  "s_addc_u32 s61, s73, 0\n\t"
__device__ __forceinline__ void stream_steps(const unsigned (&v)[4], const char* p, int& t, int tend, int& w, int& pub, int& sch,
                                             unsigned a_free, unsigned a_full) {
  unsigned vf, vt, ve;
  asm volatile(
      "s_mov_b64 s[72:73], %[p]\n\t"
      STEPADDR
      "s_getpc_b64 s[62:63]\n"
      "0:\n\t"
      "s_add_u32 s62, s62, 9f-0b-8*" STR(WMIN) "\n\t"
      "s_addc_u32 s63, s63, 0\n\t"
      "s_mov_b32 s74, 0x100000\n"
      "1:\n\t"
      "s_memrealtime s[64:65]\n\t"
      "ds_read_b32 %[vf], %[af]\n\t"
      "s_lshl_b32 s66, %[w], 3\n\t"
      "s_add_u32 s70, s62, s66\n\t"
      "s_addc_u32 s71, s63, 0\n\t"
      "s_lshl_b32 s66, %[t], 14\n\t"
      "s_and_b32 m0, s66, 0x1c000\n\t"
      "s_swappc_b64 s[68:69], s[70:71]\n\t"
      DMA4("%[v0]")
      "s_addk_i32 m0, 0x1000\n\t"
      "s_swappc_b64 s[68:69], s[70:71]\n\t"
      DMA4("%[v1]")
      "s_addk_i32 m0, 0x1000\n\t"
      "s_swappc_b64 s[68:69], s[70:71]\n\t"
      DMA4("%[v2]")
      "s_addk_i32 m0, 0x1000\n\t"
      "s_swappc_b64 s[68:69], s[70:71]\n\t"
      DMA4("%[v3]")
      "s_lshl_b32 s66, %[t], 4\n\t"
      "s_add_i32 s66, s66, 12\n\t"
      "s_sub_i32 s66, s66, %[w]\n\t"
      "s_ashr_i32 s66, s66, 4\n\t"
      "s_max_i32 %[pub], %[pub], s66\n\t"
      "v_mov_b32 %[vt], %[pub]\n\t"
      "s_waitcnt lgkmcnt(0)\n\t"
      "v_readlane_b32 s65, %[vf], 63\n\t"
      "s_lshl_b32 s64, s64, 7\n\t"
      "s_sub_i32 s64, s64, %[sch]\n\t"
      "v_mov_b32 %[ve], s64\n\t"
      "s_add_i32 %[sch], %[sch], " STR(TSCHED) "\n\t"
      "ds_write2_b32 %[al], %[vt], %[ve] offset1:64\n\t"
      "s_sub_i32 s64, s64, s65\n\t"
      "s_ashr_i32 s64, s64, 4\n\t"
      "s_mulk_i32 s64, " STR(GAIN) "\n\t"
      "s_ashr_i32 s64, s64, 11\n\t"
      "s_add_i32 s64, s64, " STR(W0) "\n\t"
      "s_max_i32 s64, s64, " STR(WMIN) "\n\t"
      "s_min_i32 %[w], s64, " STR(WMAX) "\n\t"
      "s_add_i32 %[t], %[t], 1\n\t"
      STEPADDR
      "s_add_i32 s66, %[t], -7\n"
      "2:\n\t"
      "v_cmp_gt_i32 s[64:65], s66, %[vf]\n\t"
      "s_cmp_lg_u32 s64, 0\n\t"
      "s_cbranch_scc1 3f\n"
      "4:\n\t"
      "s_cmp_lg_u32 %[t], %[tend]\n\t"
      "s_cbranch_scc1 1b\n\t"
      "s_branch 8f\n"
      "3:\n\t"
      "s_cmp_eq_u32 s74, 0\n\t"
      "s_cbranch_scc1 4b\n\t"
      "s_sub_u32 s74, s74, 1\n\t"
      "ds_read_b32 %[vf], %[af]\n\t"
      "s_waitcnt lgkmcnt(0)\n\t"
      "s_branch 2b\n"
      "9:\n\t"
      ".set i_, " STR(WMIN) "\n\t.rept " STR(WMAX) "-" STR(WMIN) "+1\n\ts_waitcnt vmcnt(i_)\n\ts_setpc_b64 s[68:69]\n\t.set i_, i_+1\n\t.endr\n"
      "8:"
      : [t] "+s"(t), [w] "+s"(w), [pub] "+s"(pub), [sch] "+s"(sch), [vf] "=&v"(vf), [vt] "=&v"(vt), [ve] "=&v"(ve)
      : [p] "s"(p), [tend] "s"(tend), [af] "v"(a_free), [al] "v"(a_full), [v0] "v"(v[0]), [v1] "v"(v[1]), [v2] "v"(v[2]), [v3] "v"(v[3])
      : "memory", "scc", "s60", "s61", "s62", "s63", "s64", "s65", "s66", "s67", "s68", "s69", "s70", "s71", "s72", "s73", "s74");
}

__device__ __forceinline__ void pairrows(const u32x4 (&raw)[8], u32x4 (&f)[8]) {
#pragma unroll
  for (int jp = 0; jp < 4; ++jp)
#pragma unroll
    for (int d = 0; d < 4; ++d) {
      f[2 * d][jp] = __builtin_amdgcn_perm(raw[2 * jp + 1][d], raw[2 * jp][d], 0x05040100u);
      f[2 * d + 1][jp] = __builtin_amdgcn_perm(raw[2 * jp + 1][d], raw[2 * jp][d], 0x07060302u);
    }
}
__device__ __forceinline__ void pin8(u32x4 (&f)[8]) {
  asm volatile("" : "+v"(f[0]), "+v"(f[1]), "+v"(f[2]), "+v"(f[3]), "+v"(f[4]), "+v"(f[5]), "+v"(f[6]), "+v"(f[7]));
}
__device__ __forceinline__ int tile_voff(int lane) { return ((32 * ((lane >> 3) & 1) + 8 * (lane >> 4)) * D + 8 * (lane & 7)) * 2; }
__device__ __forceinline__ void ldtile(u32x4 (&raw)[8], __amdgpu_buffer_rsrc_t r, int elem0, int voff) {
#pragma unroll
  for (int j = 0; j < 8; ++j) raw[j] = __builtin_amdgcn_raw_buffer_load_b128(r, voff, (elem0 + j * D) * 2, AUX_NT);
}
__device__ __forceinline__ int ccol(int n) { return 8 * (n & 7) + 4 * (n >> 3); }

__device__ __forceinline__ void mld4(u32x4& v, u64 m, unsigned voff, const unsigned char* sb) {
  asm volatile("s_mov_b64 exec, %1\n\tglobal_load_dwordx4 %0, %2, %3 sc1\n\ts_mov_b64 exec, -1" : "+v"(v) : "s"(m), "v"(voff), "s"(sb) : "memory");
}
__device__ __forceinline__ unsigned umax4(u32x4 v) { return max(max(v[0], v[1]), max(v[2], v[3])); }
__device__ __forceinline__ void gather4(u32x4 (&v)[4], unsigned off, const unsigned char* base, int stride, u64 m3 = ~0ull) {
  asm volatile("global_load_dwordx4 %0, %4, %5 sc1\n\t"
               "global_load_dwordx4 %1, %4, %6 sc1\n\t"
               "global_load_dwordx4 %2, %4, %7 sc1\n\t"
               "s_mov_b64 exec, %9\n\t"
               "global_load_dwordx4 %3, %4, %8 sc1\n\t"
               "s_mov_b64 exec, -1\n\t"
               "s_waitcnt vmcnt(0)"
               : "=&v"(v[0]), "=&v"(v[1]), "=&v"(v[2]), "+v"(v[3])
               : "v"(off), "s"(base), "s"(base + stride), "s"(base + 2 * stride), "s"(base + 3 * stride), "s"(m3) : "memory");
  const unsigned a = max(max(max(v[0][0], v[0][1]), v[0][2]), max(max(v[0][3], v[1][0]), v[1][1]));
  const unsigned b = max(max(max(v[1][2], v[1][3]), v[2][0]), max(max(v[2][1], v[2][2]), v[2][3]));
  if (__builtin_amdgcn_ballot_w64(max(max(a, b), umax4(v[3])) == SENT) == 0) return;
#pragma unroll 1
  for (unsigned spins = 0; spins < SPIN_CAP; ++spins) {
    u64 mk[4];
#pragma unroll
    for (int i = 0; i < 4; ++i) mk[i] = __builtin_amdgcn_ballot_w64(umax4(v[i]) == SENT);
    if (((mk[0] | mk[1]) | (mk[2] | mk[3])) == 0) break;
#pragma unroll
    for (int i = 0; i < 4; ++i) mld4(v[i], mk[i], off, base + i * stride);
    asm volatile("s_waitcnt vmcnt(0)" : "+v"(v[0]), "+v"(v[1]), "+v"(v[2]), "+v"(v[3])::"memory");
  }
}
__device__ __forceinline__ f32x4 sum4(const u32x4 (&v)[4]) {
  return (__builtin_bit_cast(f32x4, v[0]) + __builtin_bit_cast(f32x4, v[1])) + (__builtin_bit_cast(f32x4, v[2]) + __builtin_bit_cast(f32x4, v[3]));
}
#define ONE_PAGE(LEN) asm volatile("s_branch 1f\n\t.p2align 12,," #LEN "\n1:")

__global__ void __launch_bounds__(NTHR, 3)
fused_block(const bf16_t* __restrict__ x, const bf16_t* __restrict__ kc, const bf16_t* __restrict__ vc,
            const bf16_t* __restrict__ wq, const bf16_t* __restrict__ wo, const bf16_t* __restrict__ wg,
            const bf16_t* __restrict__ wu, const bf16_t* __restrict__ wd, bf16_t* __restrict__ out, unsigned char* ws) {
  const int tid = threadIdx.x;
  {
    unsigned v0, v1;
    asm volatile(
        "v_readfirstlane_b32 s62, %[tid]\n\t"
        "s_lshr_b32 s63, %[g], 6\n\t"
        "s_cmp_eq_u32 s63, 2\n\t"
        "s_cselect_b32 s62, 64, s62\n\t"
        "s_cmp_gt_u32 s62, 63\n\t"
        "s_cbranch_scc1 2f\n\t"
        "s_load_dwordx2 s[60:61], %[ka], 0x8\n\t"
        "v_lshlrev_b32 %[v0], 4, %[tid]\n\t"
        "s_lshr_b32 s63, %[g], 3\n\t"
        "s_add_u32 s63, s63, %[g]\n\t"
        "s_and_b32 s63, s63, 7\n\t"
        "s_andn2_b32 s62, %[g], 7\n\t"
        "s_or_b32 s62, s62, s63\n\t"
        "s_lshl_b32 s62, s62, 20\n\t"
        "s_mov_b32 m0, 0\n\t"
        "s_waitcnt lgkmcnt(0)\n\t"
        "s_add_u32 s60, s60, s62\n\t"
        "s_addc_u32 s61, s61, 0\n"
        "1:\n\t"
        "s_brev_b32 s62, m0\n\t"
        "s_lshr_b32 s62, s62, 4\n\t"
        "v_or_b32 %[v1], s62, %[v0]\n\t"
        DMA4("%[v1]")
        "s_addk_i32 m0, 0x1000\n\t"
        "s_bitcmp0_b32 m0, 15\n\t"
        "s_cbranch_scc1 1b\n"
        "2:"
        : [v0] "=&v"(v0), [v1] "=&v"(v1)
        : [ka] "s"(__builtin_amdgcn_kernarg_segment_ptr()), [tid] "v"(tid), [g] "s"(blockIdx.x)
        : "memory", "scc", "s60", "s61", "s62", "s63");
  }
  const int lane = tid & 63, wave = __builtin_amdgcn_readfirstlane(tid >> 6);
  const int g = blockIdx.x;
  const int gi = g >> 3;
  const int gl = (g & ~7) | ((g + gi) & 7);
  const bool qprod = (g >> 6) == 2;
  if (wave != 0 && wave != 9 && (wave <= 4 || !qprod)) bar0();
  int wave0 = wave;
  asm volatile("" : "+s"(wave0));
  if (wave0 == 0) {
    __builtin_amdgcn_s_setprio(3);
    unsigned vo[4];
#pragma unroll
    for (int i = 0; i < 4; ++i) vo[i] = lane * 16 + ((i & 1) << 15) + ((i >> 1) << 14);
    const unsigned a_free = lane < 32 ? L_FREE + (lane & 3) * 4 : L_CORR, a_full = L_FULL + lane * 4;
    const size_t chunk = (size_t)gl << 20;
    const char* p = (const char*)kc + chunk;
    int t = qprod ? 0 : 2, pub = 0, w = qprod ? WMIN : WSTART, sch = t * TSCHED;
    *LP(unsigned, L_CTL + lane * 4) = lane == (L_CORR - L_CTL) / 4 ? now128() + (qprod ? 1000 * 128 : 0) : 0u;
    bar();
#pragma unroll 1
    for (int tend = NPH; tend <= NSTEP; tend += NPH) {
      stream_steps(vo, p, t, tend, w, pub, sch, a_free, a_full);
      p = (const char*)vc + chunk;
#pragma unroll
      for (int i = 0; i < 4; ++i) vo[i] ^= (lane >> 5) << 7;
    }
    waitv<32>(); LV(int, a_full) = NSTEP - 2;
    waitv<16>(); LV(int, a_full) = NSTEP - 1;
    waitv<12>(); LV(int, a_full) = NSTEP;
    waitv<8>();  LV(int, a_full) = NSTEP + 1;
    waitv<4>();  LV(int, a_full) = NSTEP + 2;
    waitv<0>();  LV(int, a_full) = NSTEP + 3;
    asm volatile("s_endpgm" ::: "memory");
  }
  const __amdgpu_buffer_rsrc_t rs = __builtin_amdgcn_make_buffer_rsrc(ws, 0, W_END, 0x00020000);
  if (wave == 9) {
    __builtin_amdgcn_raw_buffer_store_b128(u32x4{SENT, SENT, SENT, SENT}, rs, (lane < 32 ? W_RECA : W_RECB - 512) + gl * 512 + lane * 16, 0, AUX_SC1);
    if (!qprod) bar0();
  }
  const int n = lane & 15, kg = lane >> 4;
  if (wave <= 4) {
    const int ci = wave - 1;
    if (wave == 1) *LP(unsigned, L_ZERO + lane * 4) = 0u;
    unsigned full = 0;
    {
      const int lo = (8 * ci + kg) * 512 + 16 * n;
      auto fetch = [&](int t, u32x4 (&d)[4]) {
        if ((int)full <= t) full = lds_wait<1>(L_FULL, t + 1);
        const unsigned sb = (t & (NSLOT - 1)) * SLOT_BYTES + lo;
        d[0] = *LP(u32x4, sb); d[1] = *LP(u32x4, sb + 256); d[2] = *LP(u32x4, sb + 2048); d[3] = *LP(u32x4, sb + 2304);
        waitl();
        LV(unsigned, L_FREE + ci * 4) = t + 1;
        asm volatile("" : "+v"(d[0]), "+v"(d[1]), "+v"(d[2]), "+v"(d[3])::"memory");
      };
      u32x4 pre[PRE][4];
#pragma unroll
      for (int t = 0; t < PRE; ++t) fetch(t, pre[t]);
      lds_wait<1>(L_QRDY, 1u);
      const u32x4 q0 = *LP(u32x4, L_Q + 16 * n), q1 = *LP(u32x4, L_Q + 256 + 16 * n);
      constexpr float C2 = 0.0625f * 1.4426950408889634f;
      float m = -INFINITY;
      const unsigned sp = L_P + (8 * ci + kg + 4 * (n & 1)) * 4;
      auto score = [&](int t, const u32x4 (&k)[4]) {
        float s0 = 0.f, s1 = 0.f;
#pragma unroll
        for (int j = 0; j < 4; ++j) { s0 = dot2(k[0][j], q0[j], s0); s1 = dot2(k[2][j], q0[j], s1); }
#pragma unroll
        for (int j = 0; j < 4; ++j) { s0 = dot2(k[1][j], q1[j], s0); s1 = dot2(k[3][j], q1[j], s1); }
        s0 += dpp<XOR1>(s0); s1 += dpp<XOR1>(s1);
        float s = (n & 1) ? s1 : s0;
        s += dpp<XOR2>(s); s += dpp<ROR4>(s); s += dpp<ROR8>(s);
        s = rbf(s) * C2;
        m = fmaxf(m, s);
        if (n < 2) *LP(float, sp + t * 128) = s;
      };
      score(0, pre[0]);
#pragma unroll 1
      for (int t = PRE; t < NPH; ++t) { u32x4 k[4]; fetch(t, k); score(t, k); }
#pragma unroll
      for (int t = 1; t < PRE; ++t) score(t, pre[t]);
      m = x32_max(x16_max(fmaxf(m, dpp<XOR1>(m))));
      if (lane == 0) *LP(float, L_MW + ci * 4) = m;
      lds_arrive(L_C1, lane);
      lds_wait<0>(L_C1, 4u);
      float M = max4(*LP(f32x4, L_MW));
      M = M == -INFINITY ? 0.f : M;
      const unsigned pp = L_P + (ci * 64 + lane) * 32;
      const f32x4 e0 = *LP(f32x4, pp), e1 = *LP(f32x4, pp + 16);
      u32x4 hi, lo2;
      float l = 0.f;
#pragma unroll
      for (int j = 0; j < 4; ++j) {
        const float a = __builtin_amdgcn_exp2f((j < 2 ? e0[2 * j] : e1[2 * j - 4]) - M);
        const float b = __builtin_amdgcn_exp2f((j < 2 ? e0[2 * j + 1] : e1[2 * j - 3]) - M);
        l += a + b;
        hi[j] = pk2(a, b);
        lo2[j] = pk2(a - bflo(hi[j]), b - bfhi(hi[j]));
      }
      *LP(u32x4, pp) = hi; *LP(u32x4, pp + 16) = lo2;
      l = wave_sum(l);
      if (lane == 0) *LP(float, L_LW + ci * 4) = l;
      lds_arrive(L_C2, lane);
      lds_wait<0>(L_C2, 4u);
    }
    const int q = n >> 2;
    const int lo = q * 512 + (ci ^ (q & 1)) * 128 + kg * 32 + (n & 3) * 8;
    const bool act = q == kg && (n & 3) < 2;
    unsigned pa = act ? L_P + (n & 1) * 16 : L_ZERO;
    const unsigned pinc = act ? 128u : 0u;
    int off = (lane & 1) ? OOB : W_RECA + gl * RECA + (64 * ci + lane) * 2;
    pin(off);
    f32x4 acc = {0.f, 0.f, 0.f, 0.f};
    float sc = 0.f;
    u32x4 a[4];
    ONE_PAGE(1280);
#pragma unroll 1
    for (int t = NPH; t < NSTEP - 1; ++t) {
#pragma unroll
      for (int m = 0; m < 4; ++m) a[m] = *LP(u32x4, pa + 32 * m);
      pa += pinc;
      if (t == NSTEP - 4) { lds_wait<1>(L_SCRDY, 1u); sc = *LP(float, L_SCALE); }
      if ((int)full <= t) full = lds_wait<1>(L_FULL, t + 1);
      const unsigned sb = (t & (NSLOT - 1)) * SLOT_BYTES + lo;
      u32x2 v[8];
#pragma unroll
      for (int j = 0; j < 8; ++j) v[j] = tr_read(sb + 2048 * j);
      __builtin_amdgcn_sched_barrier(0);
#pragma unroll
      for (int m = 0; m < 4; ++m) acc = mfma(a[m], u32x4{v[2 * m][0], v[2 * m][1], v[2 * m + 1][0], v[2 * m + 1][1]}, acc);
      waitl();
      LV(unsigned, L_FREE + ci * 4) = t + 1;
    }
#pragma unroll
    for (int m = 0; m < 4; ++m) a[m] = *LP(u32x4, pa + 32 * m);
    unsigned sb = ((NSTEP - 1) & (NSLOT - 1)) * SLOT_BYTES + lo, af = L_FULL;
    asm volatile("" : "+v"(a[0]), "+v"(a[1]), "+v"(a[2]), "+v"(a[3]), "+v"(sb), "+v"(af));
#pragma unroll
    for (int m = 0; m < 4; ++m) {
      lds_spin(af, NSTEP + m);
      const u32x2 v0 = tr_read(sb + 4096 * m), v1 = tr_read(sb + 4096 * m + 2048);
      acc = mfma(a[m], u32x4{v0[0], v0[1], v1[0], v1[1]}, acc);
    }
    const float o = (acc[0] + acc[1]) * sc;
    __builtin_amdgcn_raw_buffer_store_b32(canonu(pk2(o, dpp<XOR1>(o))), rs, off, 0, AUX_SC1);
    lds_arrive(L_PUB, lane);
    return;
  }

  const int tw = wave - 5;
  const bool roleB = gi < 7, roleC = gi >= 7 && gi < 11 && (g & 7) < 7;
  const int ks = g & 7, cs = gi, jc = 4 * (g & 7) + gi - 7;
  u64 tag;
  {
    unsigned z = 0;
    pin(z);
    const u64 gen = __hip_atomic_load((GAS u64*)(ws + W_GEN + z), RLX_AGENT);
    const int qc = 64 * ((g & 7) * 8 + (gi & 7));
    if (qprod) {
      u32x4 wqr[8], xa[2], xr[2];
      const int row0 = 64 * tw + 8 * (lane >> 3);
#pragma unroll
      for (int b = 0; b < 2; ++b) { xa[b] = *(const GAS u32x4*)(x + b * D + 8 * min(lane, 55)); xr[b] = *(const GAS u32x4*)(x + b * D + row0); }
#pragma unroll
      for (int i = 0; i < 8; ++i) wqr[i] = __builtin_nontemporal_load((const GAS u32x4*)(wq + (size_t)(row0 + i) * HD + qc + 8 * (lane & 7)));
      __builtin_amdgcn_sched_barrier(0);
      bar0();
      __builtin_amdgcn_sched_barrier(0);
#pragma unroll
      for (int b = 0; b < 2; ++b) {
        if (lane >= 56) xa[b] = u32x4{0u, 0u, 0u, 0u};
        float xs[8], s = 0.f, qv = 0.f;
#pragma unroll
        for (int i = 0; i < 4; ++i) { xs[2 * i] = bflo(xa[b][i]); xs[2 * i + 1] = bfhi(xa[b][i]); s += xs[2 * i] + xs[2 * i + 1]; }
        const float mean = wave_sum(s) * (1.f / D);
#pragma unroll
        for (int i = 0; i < 8; ++i) { const float d = xs[i] - mean; qv = fmaf(d, d, qv); }
        if (lane >= 56) qv = 0.f;
        const float mb = rbf(mean), rsd = ln_rstd(wave_sum(qv) * (1.f / D));
#pragma unroll
        for (int i = 0; i < 4; ++i) {
          const unsigned d2 = pk2(bflo(xr[b][i]) - mb, bfhi(xr[b][i]) - mb);
          xr[b][i] = pk2(bflo(d2) * rsd, bfhi(d2) * rsd);
        }
      }
      u32x4 pr[8];
      pairrows(wqr, pr);
      float b8[8], b4[4];
#pragma unroll
      for (int j = 0; j < 8; ++j) {
        float a0 = 0.f, a1 = 0.f;
#pragma unroll
        for (int i = 0; i < 4; ++i) { a0 = dot2(pr[j][i], xr[0][i], a0); a1 = dot2(pr[j][i], xr[1][i], a1); }
        b8[j] = tr32(a0, a1);
      }
#pragma unroll
      for (int j = 0; j < 4; ++j) { b4[j] = tr16(b8[j], b8[j + 4]); b4[j] += dpp<ROR8>(b4[j]); }
      if ((lane & 8) == 0) *LP(f32x4, L_RED + (tw * 128 + (lane >> 5) * 64 + 8 * (lane & 7) + 4 * (kg & 1)) * 4) = f32x4{b4[0], b4[1], b4[2], b4[3]};
      lds_arrive(L_QCNT, lane);
    }
    tag = (((u64)uni((unsigned)(gen >> 32)) << 32) | uni((unsigned)gen)) + 0x9E3779B97F4A7C15ull;
    if (tw == 0) {
      const u64 t48 = tag & ~0xFFFFull;
      if (qprod) {
        lds_wait<0>(L_QCNT, 7u);
#pragma unroll
        for (int b = 0; b < 2; ++b) {
          float qs = 0.f;
#pragma unroll
          for (int w = 0; w < 7; ++w) qs += *LP(float, L_RED + (w * 128 + b * 64 + lane) * 4);
          __hip_atomic_store((GAS u64*)(ws + W_QG) + b * HD + qc + lane, t48 | f2bf(qs), RLX_AGENT);
        }
      }
      lds_wait<2>(L_FULL, qprod ? 1 : QPOLL);
      GAS u64* qg = (GAS u64*)(ws + W_QG) + gi * DH + lane;
      u64 v[4];
#pragma unroll 1
      for (unsigned spins = 0; spins < SPIN_CAP; ++spins) {
        bool ok = true;
#pragma unroll
        for (int i = 0; i < 4; ++i) v[i] = __hip_atomic_load(qg + 64 * i, RLX_AGENT);
#pragma unroll
        for (int i = 0; i < 4; ++i) ok &= (v[i] & ~0xFFFFull) == t48;
        if (__all(ok)) break;
        __builtin_amdgcn_s_sleep(4);
      }
#pragma unroll
      for (int i = 0; i < 4; ++i) *LP(unsigned short, L_Q + (64 * i + lane) * 2) = (unsigned short)v[i];
      waitl();
      LV(unsigned, L_QRDY) = 1u;
    }
  }
  if (tw == 5) {
    lds_wait<2>(L_FULL, 3u);
    LV(unsigned, L_CORR) = LV(unsigned, L_E) - (qprod ? QLAG * 128 : 0);
  }
  u32x4 wf[2][8];
  if (roleB && tw < 4) {
    const __amdgpu_buffer_rsrc_t r = __builtin_amdgcn_make_buffer_rsrc((void*)wo, 0, HD * D * 2, 0x00020000);
    lds_wait<8>(L_FULL, TW);
    u32x4 raw[2][8];
#pragma unroll
    for (int s = 0; s < 2; ++s) ldtile(raw[s], r, (512 * ks + 128 * tw + 64 * s) * D + 64 * cs, tile_voff(lane));
#pragma unroll
    for (int s = 0; s < 2; ++s) {
      u32x4 p[8];
      pairrows(raw[s], p);
#pragma unroll
      for (int c = 0; c < 4; ++c)
#pragma unroll
        for (int jp = 0; jp < 4; ++jp) {
          wf[s][c][jp] = __builtin_amdgcn_update_dpp((int)p[c][jp], (int)p[4 + c][jp], ROR8, 0xf, 0xc, false);
          wf[s][4 + c][jp] = __builtin_amdgcn_update_dpp((int)p[4 + c][jp], (int)p[c][jp], ROR8, 0xf, 0x3, false);
        }
      pin8(wf[s]);
    }
  } else if (roleC) {
    const GAS char* gp = (const GAS char*)((n & 8) ? wu : wg) + ((64 * tw + 16 * kg) * FF + 64 * jc + 8 * (n & 7)) * 2;
    lds_wait<8>(L_FULL, tw == 5 ? 6 : TW);
    u32x4 raw[2][8];
#pragma unroll
    for (int j = 0; j < 16; ++j) raw[j >> 3][j & 7] = __builtin_nontemporal_load((const GAS u32x4*)(gp + j * (FF * 2)));
    __builtin_amdgcn_sched_barrier(0);
    pairrows(raw[0], wf[0]); pin8(wf[0]);
    pairrows(raw[1], wf[1]); pin8(wf[1]);
  }
  if (tw == 5) {
    const unsigned tag32 = (unsigned)(tag >> 32);
    const int adv = roleC ? ADVC : 0;
    unsigned A = uni(LV(unsigned, L_CORR)), prev = 0, e = 0;
    int Bd = 0, tk = 0, f = 0;
#pragma unroll 1
    for (int k2 = 0; k2 <= 2 * NCP; ++k2) {
      const int k = k2 >> 1, tc = CP0 + CPS * k;
      const int until = k == NCP ? NSTEP - 5 : (k2 & 1) ? tc + CPD + (gi & 3) : tc;
#pragma unroll 1
      while (f < until) {
        f = max((int)lds_wait<4>(L_FULL, f + 1), f + 1);
        LV(unsigned, L_CORR) = A + Bd * (f - tk) - adv * f;
      }
      if (k == NCP) break;
      GAS u64* pp = (GAS u64*)(ws + W_PROG) + k * 32;
      if (!(k2 & 1)) {
        e = uni(LV(unsigned, L_E));
        if (lane == 0 && (g >> 5) == 7) __hip_atomic_store(pp + (g & 31), ((u64)tag32 << 32) | e, RLX_AGENT);
      } else {
        const u64 pv = __hip_atomic_load(pp + (lane & 31), RLX_AGENT);
        const bool ok = (unsigned)(pv >> 32) == tag32;
        const float sum = x16_sum(row16_sum(ok ? (float)(int)((unsigned)pv - e) : 0.f)), cnt = x16_sum(row16_sum(ok ? 1.f : 0.f));
        A = e + uni((unsigned)(int)(sum * __builtin_amdgcn_rcpf(fmaxf(cnt, 1.f))));
        if (k) Bd = min(BHI, (int)(A - prev) / CPS - BIAS);
        prev = A; tk = tc;
      }
    }
    LV(unsigned, L_TFIN) = A + (Bd + BIAS) * (NSTEP - tk) + NSTEP * TSCHED;
  }
  if (tw == 4) {
    lds_wait<8>(L_C2, 4u);
    const f32x4 lw = *LP(f32x4, L_LW);
    const float Mw = max4(*LP(f32x4, L_MW)), Lw = (lw[0] + lw[1]) + (lw[2] + lw[3]);
    const unsigned tlo = (unsigned)tag, thi = (unsigned)(tag >> 32);
    GAS u64* fp = (GAS u64*)(ws + W_FLAG);
    waitv<0>();
    if (lane < 2) __hip_atomic_store(fp + 2 * gl + lane, ((u64)(lane ? thi : tlo) << 32) | __float_as_uint(lane ? Lw : Mw), RLX_AGENT);
    lds_wait<16>(L_FULL, ML_STEP);
    const int nall = roleB || roleC ? 8 : 0;
    const unsigned want = (lane & 1) ? thi : tlo;
    u64 ml = 0;
#pragma unroll 1
    for (unsigned spins = 0; spins < SPIN_CAP; ++spins) {
      ml = __hip_atomic_load(fp + 16 * gi + n, RLX_AGENT);
      bool ok = (unsigned)(ml >> 32) == want;
#pragma unroll 1
      for (int i = 0; i < nall; i += 4) {
        u64 v[4];
#pragma unroll
        for (int j = 0; j < 4; ++j) v[j] = __hip_atomic_load(fp + 64 * (i + j) + lane, RLX_AGENT);
#pragma unroll
        for (int j = 0; j < 4; ++j) ok &= (unsigned)(v[j] >> 32) == want;
      }
      if (__all(ok)) break;
      __builtin_amdgcn_s_sleep(16);
    }
    const float mi = dpp<0xA0>(__uint_as_float((unsigned)ml)), li = dpp<0xF5>(__uint_as_float((unsigned)ml));
    float Mg = fmaxf(mi, dpp<XOR2>(mi)); Mg = fmaxf(Mg, dpp<HMIR>(Mg)); Mg = fmaxf(Mg, dpp<MIR>(Mg));
    float Lg = li * __builtin_amdgcn_exp2f(mi - Mg);
    Lg += dpp<XOR2>(Lg); Lg += dpp<HMIR>(Lg); Lg += dpp<MIR>(Lg);
    LV(float, L_SCALE) = __builtin_amdgcn_exp2f(Mw - Mg) / Lg;
    waitl();
    LV(unsigned, L_SCRDY) = 1u;
    if (g == 0 && lane == 0) __hip_atomic_store((GAS u64*)(ws + W_GEN), tag, RLX_AGENT);
  }

  if (!roleC) {
    if (!roleB || tw >= 4) return;
    unsigned off = W_RECA + (((kg & 1) * 16 + 2 * ks + (tw >> 1)) * 8 + (kg >> 1)) * RECA + 256 * (tw & 1) + 16 * n;
    unsigned ta = tw * 4352 + kg * 272 + 16 * n, tb = tw * 4352 + n * 272 + 16 * kg;
    unsigned wa = L_RED + ((tw * 2 + (lane >> 5)) * 64 + ccol(n) + 2 * (kg & 1)) * 4, ra = L_RED + (lane >> 5) * 1024 + (tw * 32 + (lane & 31)) * 4;
    int so = lane < 32 ? W_RECB + ((ks * 2 + (tw >> 1)) * D + 64 * cs + 32 * (tw & 1) + lane) * 4 : OOB;
    u32x4 v[4];
    v[3] = u32x4{0u, 0u, 0u, 0u};
    pin(v[3]); pin(off); pin(ta); pin(tb); pin(wa); pin(ra); pin(so);
    lds_wait<8>(L_SCRDY, 1u);
    ONE_PAGE(1280);
    lds_wait<8>(L_FULL, NSTEP - 3);
    lds_wait<1>(L_PUB, 4u);
    wait_expected(BX);
    gather4(v, off, ws, 2 * RECA);
#pragma unroll
    for (int i = 0; i < 4; ++i) *LP(u32x4, ta + i * 1088) = v[i];
    asm volatile("" ::: "memory");
#pragma unroll
    for (int i = 0; i < 4; ++i) v[i] = *LP(u32x4, tb + i * 64);
    __builtin_amdgcn_sched_barrier(0);
    f32x4 d[4];
#pragma unroll
    for (int c = 0; c < 4; ++c) d[c] = mfma(v[0], wf[0][c], f32x4{0.f, 0.f, 0.f, 0.f});
#pragma unroll
    for (int c = 0; c < 4; ++c) d[c] = mfma(v[1], wf[0][4 + c], d[c]);
    __builtin_amdgcn_sched_barrier(0);
#pragma unroll
    for (int s = 2; s < 4; ++s)
#pragma unroll
      for (int c = 0; c < 4; ++c) d[c] = mfma(v[s], wf[1][4 * (s & 1) + c], d[c]);
    __builtin_amdgcn_sched_barrier(0);
    float e[4];
#pragma unroll
    for (int c = 0; c < 4; ++c) e[c] = tr32(d[c][0] + d[c][2], d[c][1] + d[c][3]);
    *LP(f32x2, wa) = f32x2{tr16(e[0], e[2]), tr16(e[1], e[3])};
    bar();
    __builtin_amdgcn_raw_buffer_store_b32(canon(x32_sum(*LP(float, ra) + *LP(float, ra + 512))), rs, so, 0, AUX_SC1);
    return;
  }

  const int b = (n >> 2) & 1;
  const int col = 64 * tw + 16 * kg + 4 * (n & 3);
  const u32x2 xp = *(const GAS u32x2*)(x + b * D + col);
  f32x4 xf = {bflo(xp[0]), bfhi(xp[0]), bflo(xp[1]), bfhi(xp[1])};
  unsigned off = W_RECB + (((n & 8) + b) * D + col) * 4;
  unsigned a_h = n < 8 ? L_H + (b * D + col) * 2 : 32768 + tw * 512 + lane * 8;
  unsigned a_st = (lane & 0x1B) == 0 ? L_ST + (b * 8 + tw) * 8 + (lane >> 5) * 4 : 16384 + tw * 256 + lane * 4;
  unsigned a_red = n < 12 ? L_RED + tw * 1536 + (n >> 2) * 512 + (kg >> 1) * 256 + (kg & 1) * 16 + (n & 3) * 32 : tw * 2048 + lane * 16;
  unsigned a_rd = L_RED + tw * 512 + lane * 4, a_rc = L_RED + 1024 + lane * 4, a_sr = L_ST + (tw * 8 + (lane & 7)) * 8;
  unsigned a_g = L_G + (tw * 64 + 8 * (lane & 7) + (lane >> 3)) * 2, a_gr = L_G + (b * 64 + 32 * (n & 1) + 8 * kg) * 2;
  int so = lane < 32 ? W_RECC + ((jc * 2 + kg) * D + 64 * tw + ccol(n)) * 4 : OOB;
  unsigned msel = (lane & 8) ? ~0u : 0u;
  const int oc = 32 * jc + 4 * (lane & 7);
  unsigned offd = W_RECC + (lane >> 3) * (2 * D * 4) + oc * 4, a_hd = L_H + (oc + 2 * (lane >> 5)) * 2;
  int oo = (lane & 24) ? OOB : (oc + 2 * (lane >> 5)) * 2, vd = tile_voff(lane);
  constexpr unsigned ONE2 = 0x3F803F80u;
  u32x4 v[4], w[4], b0 = {ONE2, ONE2, ONE2, ONE2}, b1 = b0;
  v[3] = u32x4{0u, 0u, 0u, 0u}; w[3] = v[3];
  pin(v[3]); pin(w[3]); pin(b0); pin(b1); pin(xf); pin(off); pin(a_h); pin(a_st); pin(a_red); pin(a_rd); pin(a_rc); pin(a_sr);
  pin(a_g); pin(a_gr); pin(so); pin(msel); pin(offd); pin(a_hd); pin(oo); pin(vd);
  const __amdgpu_buffer_rsrc_t rd = __builtin_amdgcn_make_buffer_rsrc((void*)wd, 0, FF * D * 2, 0x00020000);
  const __amdgpu_buffer_rsrc_t ro = __builtin_amdgcn_make_buffer_rsrc((void*)out, 0, 2 * D * 2, 0x00020000);
  lds_wait<8>(L_SCRDY, 1u);
  ONE_PAGE(3840);
  lds_wait<8>(L_FULL, NSTEP - 3);
  lds_wait<1>(L_PUB, 4u);
  u32x4 fd[8];
  {
    u32x4 raw[8];
    ldtile(raw, rd, 64 * jc * D + 64 * tw, vd);
    pairrows(raw, fd); pin8(fd);
  }
  wait_expected(CX);
  gather4(v, off, ws, 2 * D * 4);
  {
    f32x4 a = sum4(v);
#pragma unroll
    for (int j = 0; j < 4; ++j) { float t = a[j] + dpp<ROR8>(a[j]); pin(t); a[j] = t; }
    unsigned a01 = pk2(a[0], a[1]), a23 = pk2(a[2], a[3]);
    asm volatile("" : "+v"(a01), "+v"(a23));
    const unsigned h0 = pk2(xf[0] + bflo(a01), xf[1] + bfhi(a01)), h1 = pk2(xf[2] + bflo(a23), xf[3] + bfhi(a23));
#define ACT(o, v, ctrl) o = (unsigned)__builtin_amdgcn_update_dpp((int)(o), (int)(v), ctrl, 0xf, 0x3, false)
    ACT(b0[0], h0, 0x00); ACT(b0[1], h1, 0x00); ACT(b0[2], h0, 0x55); ACT(b0[3], h1, 0x55);
    ACT(b1[0], h0, 0xAA); ACT(b1[1], h1, 0xAA); ACT(b1[2], h0, 0xFF); ACT(b1[3], h1, 0xFF);
    *LP(u32x2, a_h) = u32x2{h0, h1};
    f32x4 r[2];
#pragma unroll
    for (int c = 0; c < 8; ++c) {
      const f32x4 d = mfma(wf[1][c], b1, mfma(wf[0][c], b0, f32x4{0.f, 0.f, 0.f, 0.f}));
      if ((c & 3) == 0) r[c >> 2] = d;
      else {
        const bool take = __builtin_amdgcn_inverse_ballot_w64(0x1111111111111111ull << (c & 3));
#pragma unroll
        for (int j = 0; j < 4; ++j) r[c >> 2][j] = take ? d[j] : r[c >> 2][j];
      }
      if (c == 3) *LP(f32x4, a_red) = r[0];
    }
    float s1 = x16_sum(tr32(dot2(h1, ONE2, dot2(h0, ONE2, 0.f)), dot2(h1, h1, dot2(h0, h0, 0.f))));
    s1 += dpp<XOR1>(s1); s1 += dpp<XOR2>(s1);
    *LP(float, a_st) = s1;
    *LP(f32x4, a_red + 128) = r[1];
  }
  bar();
  if (tw < 2) {
    f32x2 st = *LP(f32x2, a_sr);
    f32x2 ps[7], pc[7];
#pragma unroll
    for (int i = 0; i < 7; ++i) {
      ps[i] = f32x2{*LP(float, a_rd + i * 1536), *LP(float, a_rd + i * 1536 + 256)};
      pc[i] = f32x2{*LP(float, a_rc + i * 1536), *LP(float, a_rc + i * 1536 + 256)};
    }
    __builtin_amdgcn_sched_barrier(0);
#pragma unroll
    for (int j = 0; j < 2; ++j) { st[j] += dpp<XOR1>(st[j]); st[j] += dpp<XOR2>(st[j]); st[j] += dpp<HMIR>(st[j]); }
    const float mean = st[0] * (1.f / D);
    const float mb = rbf(mean), rsd = ln_rstd(fmaxf(fmaf(-mean, mean, st[1] * (1.f / D)), 0.f));
    const f32x2 sm = ((ps[0] + ps[1]) + (ps[2] + ps[3])) + ((ps[4] + ps[5]) + ps[6]);
    const f32x2 cm = ((pc[0] + pc[1]) + (pc[2] + pc[3])) + ((pc[4] + pc[5]) + pc[6]);
    const unsigned gu = pk2((sm[0] - mb * cm[0]) * rsd, (sm[1] - mb * cm[1]) * rsd);
    const float xg = bflo(gu);
    constexpr float K1 = -2.f * 0.7978845608028654f * 1.4426950408889634f;
    const float ge = xg * __builtin_amdgcn_rcpf(1.f + __builtin_amdgcn_exp2f(xg * fmaf(xg * xg, K1 * 0.044715f, K1)));
    *LP(unsigned short, a_g) = (unsigned short)f2bf(rbf(ge) * bfhi(gu));
  }
  bar();
  {
    const u32x4 a0 = *LP(u32x4, a_gr);
    f32x4 d[8];
#pragma unroll
    for (int c = 0; c < 8; ++c) d[c] = mfma(a0, fd[c], f32x4{0.f, 0.f, 0.f, 0.f});
    __builtin_amdgcn_sched_barrier(0);
    u32x4 o;
#pragma unroll
    for (int c = 0; c < 4; ++c) {
      const unsigned keep = (__float_as_uint(d[c + 4][1]) & msel) | (__float_as_uint(d[c][0]) & ~msel);
      const unsigned send = (__float_as_uint(d[c][1]) & msel) | (__float_as_uint(d[c + 4][0]) & ~msel);
      o[c] = canon(__uint_as_float(keep) + dpp<ROR8>(__uint_as_float(send)));
    }
    __builtin_amdgcn_raw_buffer_store_b128(o, rs, so, 0, AUX_SC1);
  }
  if (tw == 6) {
    const unsigned hv = *LP(unsigned, a_hd);
    __builtin_amdgcn_s_sleep(DDELAY);
    gather4(w, offd, ws, 8 * (2 * D * 4), 0xFFFFFFFFull);
    const f32x4 t = sum4(w);
    float s0 = x16_sum(tr32(t[0], t[2])), s1 = x16_sum(tr32(t[1], t[3]));
    s0 += dpp<ROR8>(s0); s1 += dpp<ROR8>(s1);
    const unsigned dn = pk2(s0, s1);
    __builtin_amdgcn_raw_buffer_store_b32(pk2(bflo(hv) + bflo(dn), bfhi(hv) + bfhi(dn)), ro, oo, 0, 0);
  }
}

extern "C" void kernel_launch(void* const* d_in, const int* in_sizes, int n_in, void* d_out, int out_size, void* d_ws,
                              size_t ws_size, hipStream_t stream) {
  (void)hipFuncSetAttribute((const void*)fused_block, hipFuncAttributeMaxDynamicSharedMemorySize, LDS_BYTES);
  hipLaunchKernelGGL(fused_block, dim3(NWG), dim3(NTHR), LDS_BYTES, stream, (const bf16_t*)d_in[0], (const bf16_t*)d_in[1],
                     (const bf16_t*)d_in[2], (const bf16_t*)d_in[3], (const bf16_t*)d_in[4], (const bf16_t*)d_in[6],
                     (const bf16_t*)d_in[7], (const bf16_t*)d_in[8], (bf16_t*)d_out, (unsigned char*)d_ws);
}
```

```cpp
#include <hip/hip_runtime.h>
#include <hip/hip_bf16.h>
#include <stdint.h>

typedef unsigned short bf16_t;
typedef unsigned long long u64;
typedef unsigned u32x4 __attribute__((ext_vector_type(4)));
typedef unsigned u32x2 __attribute__((ext_vector_type(2)));
typedef float f32x4 __attribute__((ext_vector_type(4)));
typedef float f32x2 __attribute__((ext_vector_type(2)));
typedef short s16x4 __attribute__((ext_vector_type(4)));
typedef __bf16 bf16x2 __attribute__((ext_vector_type(2)));
typedef __bf16 bf16x8 __attribute__((ext_vector_type(8)));
#define LAS __attribute__((address_space(3)))
#define GAS __attribute__((address_space(1)))
#define LP(T, a) ((LAS T*)(size_t)(a))
#define LV(T, a) (*(volatile LAS T*)(size_t)(a))
#define RLX_AGENT __ATOMIC_RELAXED, __HIP_MEMORY_SCOPE_AGENT
#define RLX_WG __ATOMIC_RELAXED, __HIP_MEMORY_SCOPE_WORKGROUP
#define STR2(x) #x
#define STR(x) STR2(x)

constexpr int D = 448, DH = 256, HD = 4096, FF = 1792;
constexpr int NWG = 256, NTHR = 768;
constexpr int NPH = 64, NSTEP = 2 * NPH;
constexpr int NSLOT = 8, SLOT_BYTES = 16384;
#define W0 18
#define WMIN 15
#define WMAX 32
#define GAIN 10
#define TSCHED 6940
constexpr int WSTART = 22;
constexpr int PRE = 9;
constexpr int NCP = 5, CP0 = 14, CPS = 25, CPD = 4;
constexpr int BIAS = 60, BHI = 200;
constexpr int QLAG = 150, ADVC = 80;
constexpr int QPOLL = 5, TW = 20, ML_STEP = 96;
constexpr int BX = 114, CX = 209, DDELAY = 9;
static_assert(WMIN >= 13 && WMIN <= W0 && W0 <= WMAX && WMIN <= WSTART && WSTART <= WMAX && WMAX <= 44, "window");
constexpr int L_CTL = NSLOT * SLOT_BYTES;
constexpr int L_FREE = L_CTL, L_QCNT = L_CTL + 20, L_QRDY = L_CTL + 24, L_C1 = L_CTL + 28, L_C2 = L_CTL + 32, L_SCRDY = L_CTL + 36,
              L_SCALE = L_CTL + 40, L_PUB = L_CTL + 44, L_CORR = L_CTL + 48, L_TFIN = L_CTL + 52, L_MW = L_CTL + 64, L_LW = L_CTL + 80;
constexpr int L_FULL = L_CTL + 128;
constexpr int L_E = L_FULL + 256;
constexpr int L_Q = L_E + 256;
constexpr int L_P = L_Q + 512;
constexpr int L_ZERO = L_P + 8192;
constexpr int L_ST = L_ZERO + 128;
constexpr int L_RED = L_ST + 128;
constexpr int L_H = L_RED + 10752;
constexpr int L_G = L_H + 1792;
constexpr int LDS_BYTES = L_G + 256;
static_assert(LDS_BYTES > 80 * 1024 && LDS_BYTES <= 160 * 1024, "one workgroup per CU");
constexpr int W_GEN = 0;
constexpr int W_FLAG = 4096;
constexpr int W_PROG = 8192;
constexpr int W_QG = 12288;
constexpr int W_RECA = W_QG + 65536, RECA = 512;
constexpr int W_RECB = W_RECA + 256 * RECA;
constexpr int W_RECC = W_RECB + 8 * 2 * D * 4;
constexpr int W_END = W_RECC + 28 * 2 * D * 4;
static_assert(W_END - W_RECB <= 256 * 512, "every workgroup invalidates 512 B of the B / C records");
constexpr unsigned SENT = 0xFFFFFFFFu;
constexpr int OOB = 0x40000000;
constexpr int AUX_SC1 = 16, AUX_NT = 2;
constexpr unsigned SPIN_CAP = 1u << 20;

__device__ __forceinline__ float bflo(unsigned v) { return __builtin_bit_cast(float, v << 16); }
__device__ __forceinline__ float bfhi(unsigned v) { return __builtin_bit_cast(float, v & 0xffff0000u); }
__device__ __forceinline__ unsigned f2bf(float f) { return __builtin_bit_cast(unsigned short, (__bf16)f); }
__device__ __forceinline__ float rbf(float f) { return bflo(f2bf(f)); }
__device__ __forceinline__ unsigned pk2(float lo, float hi) {
  bf16x2 v = {(__bf16)lo, (__bf16)hi};
  return __builtin_bit_cast(unsigned, v);
}
__device__ __forceinline__ float dot2(unsigned a, unsigned b, float acc) {
  return __builtin_amdgcn_fdot2_f32_bf16(__builtin_bit_cast(bf16x2, a), __builtin_bit_cast(bf16x2, b), acc, false);
}
template <int CTRL> __device__ __forceinline__ float dpp(float x) {
  return __builtin_bit_cast(float, __builtin_amdgcn_mov_dpp(__builtin_bit_cast(int, x), CTRL, 0xf, 0xf, true));
}
constexpr int XOR1 = 0xB1, XOR2 = 0x4E, HMIR = 0x141, MIR = 0x140, ROR4 = 0x124, ROR8 = 0x128;
__device__ __forceinline__ float row16_sum(float x) {
  x += dpp<XOR1>(x); x += dpp<XOR2>(x); x += dpp<HMIR>(x); x += dpp<MIR>(x);
  return x;
}
__device__ __forceinline__ float tr32(float a, float b) {
  auto s = __builtin_amdgcn_permlane32_swap(__float_as_uint(a), __float_as_uint(b), false, false);
  return __uint_as_float(s[0]) + __uint_as_float(s[1]);
}
__device__ __forceinline__ float tr16(float a, float b) {
  auto s = __builtin_amdgcn_permlane16_swap(__float_as_uint(a), __float_as_uint(b), false, false);
  return __uint_as_float(s[0]) + __uint_as_float(s[1]);
}
__device__ __forceinline__ float x16_sum(float x) { return tr16(x, x); }
__device__ __forceinline__ float x32_sum(float x) { return tr32(x, x); }
__device__ __forceinline__ float x16_max(float x) {
  auto s = __builtin_amdgcn_permlane16_swap(__float_as_uint(x), __float_as_uint(x), false, false);
  return fmaxf(__uint_as_float(s[0]), __uint_as_float(s[1]));
}
__device__ __forceinline__ float x32_max(float x) {
  auto s = __builtin_amdgcn_permlane32_swap(__float_as_uint(x), __float_as_uint(x), false, false);
  return fmaxf(__uint_as_float(s[0]), __uint_as_float(s[1]));
}
__device__ __forceinline__ float wave_sum(float x) { return x32_sum(x16_sum(row16_sum(x))); }
__device__ __forceinline__ float max4(f32x4 v) { return fmaxf(fmaxf(v[0], v[1]), fmaxf(v[2], v[3])); }
__device__ __forceinline__ float ln_rstd(float var) { return __builtin_amdgcn_rcpf(rbf(__builtin_amdgcn_sqrtf(rbf(rbf(var) + 1e-5f)))); }
__device__ __forceinline__ unsigned canonu(unsigned u) { return min(u, SENT - 1u); }
__device__ __forceinline__ unsigned canon(float f) { return canonu(__float_as_uint(f)); }
__device__ __forceinline__ f32x4 mfma(u32x4 a, u32x4 b, f32x4 c) {
  return __builtin_amdgcn_mfma_f32_16x16x32_bf16(__builtin_bit_cast(bf16x8, a), __builtin_bit_cast(bf16x8, b), c, 0, 0, 0);
}
__device__ __forceinline__ u32x2 tr_read(unsigned a) { return __builtin_bit_cast(u32x2, __builtin_amdgcn_ds_read_tr16_b64_v4i16(LP(s16x4, a))); }
__device__ __forceinline__ unsigned uni(unsigned v) { return (unsigned)__builtin_amdgcn_readfirstlane((int)v); }
__device__ __forceinline__ unsigned now128() { return (unsigned)__builtin_amdgcn_s_memrealtime() << 7; }
template <typename T> __device__ __forceinline__ void pin(T& v) { asm volatile("" : "+v"(v)); }
template <int N> __device__ __forceinline__ void waitv() { asm volatile("s_waitcnt vmcnt(%0)" ::"i"(N) : "memory"); }
__device__ __forceinline__ void waitl() { asm volatile("s_waitcnt lgkmcnt(0)" ::: "memory"); }
__device__ __forceinline__ void bar0() { __builtin_amdgcn_s_barrier(); asm volatile("" ::: "memory"); }
__device__ __forceinline__ void bar() { waitl(); bar0(); }
__device__ __forceinline__ void lds_arrive(int off, int lane) {
  waitl();
  if (lane == 0) __hip_atomic_fetch_add(LP(unsigned, off), 1u, RLX_WG);
}
template <int SLEEP> __device__ __forceinline__ unsigned lds_wait(int off, unsigned want) {
  unsigned v = 0;
#pragma unroll 1
  for (unsigned s = 0; s < SPIN_CAP; ++s) {
    v = uni(LV(unsigned, off));
    if (v >= want) break;
    if (SLEEP) __builtin_amdgcn_s_sleep(SLEEP);
  }
  asm volatile("" ::: "memory");
  return v;
}
__device__ __forceinline__ unsigned lds_spin(unsigned addr, unsigned want) {
  unsigned v, cap = SPIN_CAP;
  asm volatile("1:\n\t"
               "ds_read_b32 %0, %2\n\t"
               "s_sub_u32 %1, %1, 1\n\t"
               "s_cbranch_scc1 2f\n\t"
               "s_waitcnt lgkmcnt(0)\n\t"
               "v_cmp_gt_u32 vcc, %3, %0\n\t"
               "s_cbranch_vccnz 1b\n"
               "2:\n\t"
               "s_waitcnt lgkmcnt(0)"
               : "=&v"(v), "+s"(cap) : "v"(addr), "s"(want) : "memory", "vcc", "scc");
  return v;
}
__device__ __forceinline__ void wait_expected(int ticks) {
  const unsigned tf = uni(LV(unsigned, L_TFIN)) + ticks * 128;
#pragma unroll 1
  for (int i = 0; i < 64 && (int)(now128() - tf) < 0; ++i) __builtin_amdgcn_s_sleep(1);
}

#define DMA4(a) \
  "global_load_lds_dwordx4 " a ", s[60:61] offset:0 nt\n\t" \
  "global_load_lds_dwordx4 " a ", s[60:61] offset:1024 nt\n\t" \
  "global_load_lds_dwordx4 " a ", s[60:61] offset:2048 nt\n\t" \
  "global_load_lds_dwordx4 " a ", s[60:61] offset:3072 nt\n\t"
#define STEPADDR \
  "s_and_b32 s66, %[t], 1\n\t" \
  "s_lshl_b32 s66, s66, 13\n\t" \
  "s_bfe_u32 s67, %[t], 0x40001\n\t" \
  "s_lshl_b32 s67, s67, 16\n\t" \
  "s_or_b32 s66, s66, s67\n\t" \
  "s_bfe_u32 s67, %[t], 0x10005\n\t" \
  "s_lshl_b32 s67, s67, 12\n\t" \
  "s_or_b32 s66, s66, s67\n\t" \
  "s_add_u32 s60, s72, s66\n\t" \
  "s_addc_u32 s61, s73, 0\n\t"
#define LANDED(issued) \
  "s_lshl_b32 s66, %[t], 4\n\t" \
  "s_add_i32 s66, s66, " #issued "\n\t" \
  "s_sub_i32 s66, s66, %[w]\n\t" \
  "s_ashr_i32 s66, s66, 4\n\t" \
  "s_max_i32 %[pub], %[pub], s66\n\t" \
  "v_mov_b32 %[vt], %[pub]\n\t"
__device__ __forceinline__ void stream_steps(const unsigned (&v)[4], const char* p, int& t, int tend, int& w, int& pub, int& sch,
                                             unsigned a_free, unsigned a_full) {
  unsigned vf, vt, ve;
  asm volatile(
      "s_mov_b64 s[72:73], %[p]\n\t"
      STEPADDR
      "s_getpc_b64 s[62:63]\n"
      "0:\n\t"
      "s_add_u32 s62, s62, 9f-0b-8*" STR(WMIN) "\n\t"
      "s_addc_u32 s63, s63, 0\n\t"
      "s_mov_b32 s74, 0x100000\n"
      "1:\n\t"
      "s_memrealtime s[64:65]\n\t"
      "ds_read_b32 %[vf], %[af]\n\t"
      "s_lshl_b32 s66, %[w], 3\n\t"
      "s_add_u32 s70, s62, s66\n\t"
      "s_addc_u32 s71, s63, 0\n\t"
      "s_lshl_b32 s66, %[t], 14\n\t"
      "s_and_b32 m0, s66, 0x1c000\n\t"
      "s_swappc_b64 s[68:69], s[70:71]\n\t"
      DMA4("%[v0]")
      "s_addk_i32 m0, 0x1000\n\t"
      "s_swappc_b64 s[68:69], s[70:71]\n\t"
      DMA4("%[v1]")
      LANDED(4)
      "ds_write_b32 %[al], %[vt]\n\t"
      "s_addk_i32 m0, 0x1000\n\t"
      "s_swappc_b64 s[68:69], s[70:71]\n\t"
      DMA4("%[v2]")
      "s_addk_i32 m0, 0x1000\n\t"
      "s_swappc_b64 s[68:69], s[70:71]\n\t"
      DMA4("%[v3]")
      LANDED(12)
      "s_waitcnt lgkmcnt(0)\n\t"
      "v_readlane_b32 s65, %[vf], 63\n\t"
      "s_lshl_b32 s64, s64, 7\n\t"
      "s_sub_i32 s64, s64, %[sch]\n\t"
      "v_mov_b32 %[ve], s64\n\t"
      "s_add_i32 %[sch], %[sch], " STR(TSCHED) "\n\t"
      "ds_write2_b32 %[al], %[vt], %[ve] offset1:64\n\t"
      "s_sub_i32 s64, s64, s65\n\t"
      "s_ashr_i32 s64, s64, 4\n\t"
      "s_mulk_i32 s64, " STR(GAIN) "\n\t"
      "s_ashr_i32 s64, s64, 11\n\t"
      "s_add_i32 s64, s64, " STR(W0) "\n\t"
      "s_max_i32 s64, s64, " STR(WMIN) "\n\t"
      "s_min_i32 %[w], s64, " STR(WMAX) "\n\t"
      "s_add_i32 %[t], %[t], 1\n\t"
      STEPADDR
      "s_add_i32 s66, %[t], -7\n"
      "2:\n\t"
      "v_cmp_gt_i32 s[64:65], s66, %[vf]\n\t"
      "s_cmp_lg_u32 s64, 0\n\t"
      "s_cbranch_scc1 3f\n"
      "4:\n\t"
      "s_cmp_lg_u32 %[t], %[tend]\n\t"
      "s_cbranch_scc1 1b\n\t"
      "s_branch 8f\n"
      "3:\n\t"
      "s_cmp_eq_u32 s74, 0\n\t"
      "s_cbranch_scc1 4b\n\t"
      "s_sub_u32 s74, s74, 1\n\t"
      "ds_read_b32 %[vf], %[af]\n\t"
      "s_waitcnt lgkmcnt(0)\n\t"
      "s_branch 2b\n"
      "9:\n\t"
      ".set i_, " STR(WMIN) "\n\t.rept " STR(WMAX) "-" STR(WMIN) "+1\n\ts_waitcnt vmcnt(i_)\n\ts_setpc_b64 s[68:69]\n\t.set i_, i_+1\n\t.endr\n"
      "8:"
      : [t] "+s"(t), [w] "+s"(w), [pub] "+s"(pub), [sch] "+s"(sch), [vf] "=&v"(vf), [vt] "=&v"(vt), [ve] "=&v"(ve)
      : [p] "s"(p), [tend] "s"(tend), [af] "v"(a_free), [al] "v"(a_full), [v0] "v"(v[0]), [v1] "v"(v[1]), [v2] "v"(v[2]), [v3] "v"(v[3])
      : "memory", "scc", "s60", "s61", "s62", "s63", "s64", "s65", "s66", "s67", "s68", "s69", "s70", "s71", "s72", "s73", "s74");
}

__device__ __forceinline__ void pairrows(const u32x4 (&raw)[8], u32x4 (&f)[8]) {
#pragma unroll
  for (int jp = 0; jp < 4; ++jp)
#pragma unroll
    for (int d = 0; d < 4; ++d) {
      f[2 * d][jp] = __builtin_amdgcn_perm(raw[2 * jp + 1][d], raw[2 * jp][d], 0x05040100u);
      f[2 * d + 1][jp] = __builtin_amdgcn_perm(raw[2 * jp + 1][d], raw[2 * jp][d], 0x07060302u);
    }
}
__device__ __forceinline__ void pin8(u32x4 (&f)[8]) {
  asm volatile("" : "+v"(f[0]), "+v"(f[1]), "+v"(f[2]), "+v"(f[3]), "+v"(f[4]), "+v"(f[5]), "+v"(f[6]), "+v"(f[7]));
}
__device__ __forceinline__ int tile_voff(int lane) { return ((32 * ((lane >> 3) & 1) + 8 * (lane >> 4)) * D + 8 * (lane & 7)) * 2; }
__device__ __forceinline__ void ldtile(u32x4 (&raw)[8], __amdgpu_buffer_rsrc_t r, int elem0, int voff) {
#pragma unroll
  for (int j = 0; j < 8; ++j) raw[j] = __builtin_amdgcn_raw_buffer_load_b128(r, voff, (elem0 + j * D) * 2, AUX_NT);
}
__device__ __forceinline__ int ccol(int n) { return 8 * (n & 7) + 4 * (n >> 3); }

__device__ __forceinline__ void mld4(u32x4& v, u64 m, unsigned voff, const unsigned char* sb) {
  asm volatile("s_mov_b64 exec, %1\n\tglobal_load_dwordx4 %0, %2, %3 sc1\n\ts_mov_b64 exec, -1" : "+v"(v) : "s"(m), "v"(voff), "s"(sb) : "memory");
}
__device__ __forceinline__ unsigned umax4(u32x4 v) { return max(max(v[0], v[1]), max(v[2], v[3])); }
__device__ __forceinline__ void gather4(u32x4 (&v)[4], unsigned off, const unsigned char* base, int stride, u64 m3 = ~0ull) {
  asm volatile("global_load_dwordx4 %0, %4, %5 sc1\n\t"
               "global_load_dwordx4 %1, %4, %6 sc1\n\t"
               "global_load_dwordx4 %2, %4, %7 sc1\n\t"
               "s_mov_b64 exec, %9\n\t"
               "global_load_dwordx4 %3, %4, %8 sc1\n\t"
               "s_mov_b64 exec, -1\n\t"
               "s_waitcnt vmcnt(0)"
               : "=&v"(v[0]), "=&v"(v[1]), "=&v"(v[2]), "+v"(v[3])
               : "v"(off), "s"(base), "s"(base + stride), "s"(base + 2 * stride), "s"(base + 3 * stride), "s"(m3) : "memory");
  const unsigned a = max(max(max(v[0][0], v[0][1]), v[0][2]), max(max(v[0][3], v[1][0]), v[1][1]));
  const unsigned b = max(max(max(v[1][2], v[1][3]), v[2][0]), max(max(v[2][1], v[2][2]), v[2][3]));
  if (__builtin_amdgcn_ballot_w64(max(max(a, b), umax4(v[3])) == SENT) == 0) return;
#pragma unroll 1
  for (unsigned spins = 0; spins < SPIN_CAP; ++spins) {
    u64 mk[4];
#pragma unroll
    for (int i = 0; i < 4; ++i) mk[i] = __builtin_amdgcn_ballot_w64(umax4(v[i]) == SENT);
    if (((mk[0] | mk[1]) | (mk[2] | mk[3])) == 0) break;
#pragma unroll
    for (int i = 0; i < 4; ++i) mld4(v[i], mk[i], off, base + i * stride);
    asm volatile("s_waitcnt vmcnt(0)" : "+v"(v[0]), "+v"(v[1]), "+v"(v[2]), "+v"(v[3])::"memory");
  }
}
__device__ __forceinline__ f32x4 sum4(const u32x4 (&v)[4]) {
  return (__builtin_bit_cast(f32x4, v[0]) + __builtin_bit_cast(f32x4, v[1])) + (__builtin_bit_cast(f32x4, v[2]) + __builtin_bit_cast(f32x4, v[3]));
}
#define ONE_PAGE(LEN) asm volatile("s_branch 1f\n\t.p2align 12,," #LEN "\n1:")

__global__ void __launch_bounds__(NTHR, 3)
fused_block(const bf16_t* __restrict__ x, const bf16_t* __restrict__ kc, const bf16_t* __restrict__ vc,
            const bf16_t* __restrict__ wq, const bf16_t* __restrict__ wo, const bf16_t* __restrict__ wg,
            const bf16_t* __restrict__ wu, const bf16_t* __restrict__ wd, bf16_t* __restrict__ out, unsigned char* ws) {
  const int tid = threadIdx.x;
  {
    unsigned v0, v1;
    asm volatile(
        "v_readfirstlane_b32 s62, %[tid]\n\t"
        "s_lshr_b32 s63, %[g], 6\n\t"
        "s_cmp_eq_u32 s63, 2\n\t"
        "s_cselect_b32 s62, 64, s62\n\t"
        "s_cmp_gt_u32 s62, 63\n\t"
        "s_cbranch_scc1 2f\n\t"
        "s_load_dwordx2 s[60:61], %[ka], 0x8\n\t"
        "v_lshlrev_b32 %[v0], 4, %[tid]\n\t"
        "s_lshr_b32 s63, %[g], 3\n\t"
        "s_add_u32 s63, s63, %[g]\n\t"
        "s_and_b32 s63, s63, 7\n\t"
        "s_andn2_b32 s62, %[g], 7\n\t"
        "s_or_b32 s62, s62, s63\n\t"
        "s_lshl_b32 s62, s62, 20\n\t"
        "s_mov_b32 m0, 0\n\t"
        "s_waitcnt lgkmcnt(0)\n\t"
        "s_add_u32 s60, s60, s62\n\t"
        "s_addc_u32 s61, s61, 0\n"
        "1:\n\t"
        "s_brev_b32 s62, m0\n\t"
        "s_lshr_b32 s62, s62, 4\n\t"
        "v_or_b32 %[v1], s62, %[v0]\n\t"
        DMA4("%[v1]")
        "s_addk_i32 m0, 0x1000\n\t"
        "s_bitcmp0_b32 m0, 15\n\t"
        "s_cbranch_scc1 1b\n"
        "2:"
        : [v0] "=&v"(v0), [v1] "=&v"(v1)
        : [ka] "s"(__builtin_amdgcn_kernarg_segment_ptr()), [tid] "v"(tid), [g] "s"(blockIdx.x)
        : "memory", "scc", "s60", "s61", "s62", "s63");
  }
  const int lane = tid & 63, wave = __builtin_amdgcn_readfirstlane(tid >> 6);
  const int g = blockIdx.x;
  const int gi = g >> 3;
  const int gl = (g & ~7) | ((g + gi) & 7);
  const bool qprod = (g >> 6) == 2;
  if (wave != 0 && wave != 9 && (wave <= 4 || !qprod)) bar0();
  int wave0 = wave;
  asm volatile("" : "+s"(wave0));
  if (wave0 == 0) {
    __builtin_amdgcn_s_setprio(3);
    unsigned vo[4];
#pragma unroll
    for (int i = 0; i < 4; ++i) vo[i] = lane * 16 + ((i & 1) << 15) + ((i >> 1) << 14);
    const unsigned a_free = lane < 32 ? L_FREE + (lane & 3) * 4 : L_CORR, a_full = L_FULL + lane * 4;
    const size_t chunk = (size_t)gl << 20;
    const char* p = (const char*)kc + chunk;
    int t = qprod ? 0 : 2, pub = 0, w = qprod ? WMIN : WSTART, sch = t * TSCHED;
    *LP(unsigned, L_CTL + lane * 4) = lane == (L_CORR - L_CTL) / 4 ? now128() + (qprod ? 1000 : -150) * 128 : 0u;
    bar();
#pragma unroll 1
    for (int tend = NPH; tend <= NSTEP; tend += NPH) {
      stream_steps(vo, p, t, tend, w, pub, sch, a_free, a_full);
      p = (const char*)vc + chunk;
#pragma unroll
      for (int i = 0; i < 4; ++i) vo[i] ^= (lane >> 5) << 7;
    }
    waitv<32>(); LV(int, a_full) = NSTEP - 2;
    waitv<16>(); LV(int, a_full) = NSTEP - 1;
    waitv<12>(); LV(int, a_full) = NSTEP;
    waitv<8>();  LV(int, a_full) = NSTEP + 1;
    waitv<4>();  LV(int, a_full) = NSTEP + 2;
    waitv<0>();  LV(int, a_full) = NSTEP + 3;
    asm volatile("s_endpgm" ::: "memory");
  }
  const __amdgpu_buffer_rsrc_t rs = __builtin_amdgcn_make_buffer_rsrc(ws, 0, W_END, 0x00020000);
  if (wave == 9) {
    __builtin_amdgcn_raw_buffer_store_b128(u32x4{SENT, SENT, SENT, SENT}, rs, (lane < 32 ? W_RECA : W_RECB - 512) + gl * 512 + lane * 16, 0, AUX_SC1);
    if (!qprod) bar0();
  }
  const int n = lane & 15, kg = lane >> 4;
  if (wave <= 4) {
    const int ci = wave - 1;
    if (wave == 1) *LP(unsigned, L_ZERO + lane * 4) = 0u;
    unsigned full = 0;
    {
      const int lo = (8 * ci + kg) * 512 + 16 * n;
      auto fetch = [&](int t, u32x4 (&d)[4]) {
        if ((int)full <= t) full = lds_wait<1>(L_FULL, t + 1);
        const unsigned sb = (t & (NSLOT - 1)) * SLOT_BYTES + lo;
        d[0] = *LP(u32x4, sb); d[1] = *LP(u32x4, sb + 256); d[2] = *LP(u32x4, sb + 2048); d[3] = *LP(u32x4, sb + 2304);
        waitl();
        LV(unsigned, L_FREE + ci * 4) = t + 1;
        asm volatile("" : "+v"(d[0]), "+v"(d[1]), "+v"(d[2]), "+v"(d[3])::"memory");
      };
      u32x4 pre[PRE][4];
#pragma unroll
      for (int t = 0; t < PRE; ++t) fetch(t, pre[t]);
      lds_wait<1>(L_QRDY, 1u);
      const u32x4 q0 = *LP(u32x4, L_Q + 16 * n), q1 = *LP(u32x4, L_Q + 256 + 16 * n);
      constexpr float C2 = 0.0625f * 1.4426950408889634f;
      float m = -INFINITY;
      const unsigned sp = L_P + (8 * ci + kg + 4 * (n & 1)) * 4;
      auto score = [&](int t, const u32x4 (&k)[4]) {
        float s0 = 0.f, s1 = 0.f;
#pragma unroll
        for (int j = 0; j < 4; ++j) { s0 = dot2(k[0][j], q0[j], s0); s1 = dot2(k[2][j], q0[j], s1); }
#pragma unroll
        for (int j = 0; j < 4; ++j) { s0 = dot2(k[1][j], q1[j], s0); s1 = dot2(k[3][j], q1[j], s1); }
        s0 += dpp<XOR1>(s0); s1 += dpp<XOR1>(s1);
        float s = (n & 1) ? s1 : s0;
        s += dpp<XOR2>(s); s += dpp<ROR4>(s); s += dpp<ROR8>(s);
        s = rbf(s) * C2;
        m = fmaxf(m, s);
        if (n < 2) *LP(float, sp + t * 128) = s;
      };
      score(0, pre[0]);
#pragma unroll 1
      for (int t = PRE; t < NPH; ++t) { u32x4 k[4]; fetch(t, k); score(t, k); }
#pragma unroll
      for (int t = 1; t < PRE; ++t) score(t, pre[t]);
      m = x32_max(x16_max(fmaxf(m, dpp<XOR1>(m))));
      if (lane == 0) *LP(float, L_MW + ci * 4) = m;
      lds_arrive(L_C1, lane);
      lds_wait<0>(L_C1, 4u);
      float M = max4(*LP(f32x4, L_MW));
      M = M == -INFINITY ? 0.f : M;
      const unsigned pp = L_P + (ci * 64 + lane) * 32;
      const f32x4 e0 = *LP(f32x4, pp), e1 = *LP(f32x4, pp + 16);
      u32x4 hi, lo2;
      float l = 0.f;
#pragma unroll
      for (int j = 0; j < 4; ++j) {
        const float a = __builtin_amdgcn_exp2f((j < 2 ? e0[2 * j] : e1[2 * j - 4]) - M);
        const float b = __builtin_amdgcn_exp2f((j < 2 ? e0[2 * j + 1] : e1[2 * j - 3]) - M);
        l += a + b;
        hi[j] = pk2(a, b);
        lo2[j] = pk2(a - bflo(hi[j]), b - bfhi(hi[j]));
      }
      *LP(u32x4, pp) = hi; *LP(u32x4, pp + 16) = lo2;
      l = wave_sum(l);
      if (lane == 0) *LP(float, L_LW + ci * 4) = l;
      lds_arrive(L_C2, lane);
      lds_wait<0>(L_C2, 4u);
    }
    const int q = n >> 2;
    const int lo = q * 512 + (ci ^ (q & 1)) * 128 + kg * 32 + (n & 3) * 8;
    const bool act = q == kg && (n & 3) < 2;
    unsigned pa = act ? L_P + (n & 1) * 16 : L_ZERO;
    const unsigned pinc = act ? 128u : 0u;
    int off = (lane & 1) ? OOB : W_RECA + gl * RECA + (64 * ci + lane) * 2;
    pin(off);
    f32x4 acc = {0.f, 0.f, 0.f, 0.f};
    float sc = 0.f;
    u32x4 al[4];
#pragma unroll
    for (int m = 0; m < 4; ++m) al[m] = *LP(u32x4, pa + (NPH - 1) * pinc + 32 * m);
    unsigned sl = ((NSTEP - 1) & (NSLOT - 1)) * SLOT_BYTES + lo, af = L_FULL;
    asm volatile("" : "+v"(al[0]), "+v"(al[1]), "+v"(al[2]), "+v"(al[3]), "+v"(sl), "+v"(af));
    ONE_PAGE(1536);
#pragma unroll 1
    for (int t = NPH; t < NSTEP - 1; ++t) {
      u32x4 a[4];
#pragma unroll
      for (int m = 0; m < 4; ++m) a[m] = *LP(u32x4, pa + 32 * m);
      pa += pinc;
      if (t == NSTEP - 5) { lds_wait<1>(L_SCRDY, 1u); sc = *LP(float, L_SCALE); }
      if ((int)full <= t) full = uni(lds_spin(af, t + 1));
      const unsigned sb = (t & (NSLOT - 1)) * SLOT_BYTES + lo;
      u32x2 v[8];
#pragma unroll
      for (int j = 0; j < 8; ++j) v[j] = tr_read(sb + 2048 * j);
      __builtin_amdgcn_sched_barrier(0);
#pragma unroll
      for (int m = 0; m < 4; ++m) acc = mfma(a[m], u32x4{v[2 * m][0], v[2 * m][1], v[2 * m + 1][0], v[2 * m + 1][1]}, acc);
      waitl();
      LV(unsigned, L_FREE + ci * 4) = t + 1;
    }
    if (full < NSTEP + 3) full = uni(lds_spin(af, NSTEP));
    if (full >= NSTEP + 3) {
      u32x2 v[8];
#pragma unroll
      for (int j = 0; j < 8; ++j) v[j] = tr_read(sl + 2048 * j);
      __builtin_amdgcn_sched_barrier(0);
#pragma unroll
      for (int m = 0; m < 4; ++m) acc = mfma(al[m], u32x4{v[2 * m][0], v[2 * m][1], v[2 * m + 1][0], v[2 * m + 1][1]}, acc);
    } else {
#pragma unroll
      for (int m = 0; m < 4; ++m) {
        if (m) lds_spin(af, NSTEP + m);
        const u32x2 v0 = tr_read(sl + 4096 * m), v1 = tr_read(sl + 4096 * m + 2048);
        acc = mfma(al[m], u32x4{v0[0], v0[1], v1[0], v1[1]}, acc);
      }
    }
    const float o = (acc[0] + acc[1]) * sc;
    __builtin_amdgcn_raw_buffer_store_b32(canonu(pk2(o, dpp<XOR1>(o))), rs, off, 0, AUX_SC1);
    lds_arrive(L_PUB, lane);
    return;
  }

  const int tw = wave - 5;
  const bool roleB = gi < 7, roleC = gi >= 7 && gi < 11 && (g & 7) < 7;
  const int ks = g & 7, cs = gi, jc = 4 * (g & 7) + gi - 7;
  u64 tag;
  {
    unsigned z = 0;
    pin(z);
    const u64 gen = __hip_atomic_load((GAS u64*)(ws + W_GEN + z), RLX_AGENT);
    const int qc = 64 * ((g & 7) * 8 + (gi & 7));
    if (qprod) {
      u32x4 wqr[8], xa[2], xr[2];
      const int row0 = 64 * tw + 8 * (lane >> 3);
#pragma unroll
      for (int b = 0; b < 2; ++b) { xa[b] = *(const GAS u32x4*)(x + b * D + 8 * min(lane, 55)); xr[b] = *(const GAS u32x4*)(x + b * D + row0); }
#pragma unroll
      for (int i = 0; i < 8; ++i) wqr[i] = __builtin_nontemporal_load((const GAS u32x4*)(wq + (size_t)(row0 + i) * HD + qc + 8 * (lane & 7)));
      __builtin_amdgcn_sched_barrier(0);
      bar0();
      __builtin_amdgcn_sched_barrier(0);
#pragma unroll
      for (int b = 0; b < 2; ++b) {
        if (lane >= 56) xa[b] = u32x4{0u, 0u, 0u, 0u};
        float xs[8], s = 0.f, qv = 0.f;
#pragma unroll
        for (int i = 0; i < 4; ++i) { xs[2 * i] = bflo(xa[b][i]); xs[2 * i + 1] = bfhi(xa[b][i]); s += xs[2 * i] + xs[2 * i + 1]; }
        const float mean = wave_sum(s) * (1.f / D);
#pragma unroll
        for (int i = 0; i < 8; ++i) { const float d = xs[i] - mean; qv = fmaf(d, d, qv); }
        if (lane >= 56) qv = 0.f;
        const float mb = rbf(mean), rsd = ln_rstd(wave_sum(qv) * (1.f / D));
#pragma unroll
        for (int i = 0; i < 4; ++i) {
          const unsigned d2 = pk2(bflo(xr[b][i]) - mb, bfhi(xr[b][i]) - mb);
          xr[b][i] = pk2(bflo(d2) * rsd, bfhi(d2) * rsd);
        }
      }
      u32x4 pr[8];
      pairrows(wqr, pr);
      float b8[8], b4[4];
#pragma unroll
      for (int j = 0; j < 8; ++j) {
        float a0 = 0.f, a1 = 0.f;
#pragma unroll
        for (int i = 0; i < 4; ++i) { a0 = dot2(pr[j][i], xr[0][i], a0); a1 = dot2(pr[j][i], xr[1][i], a1); }
        b8[j] = tr32(a0, a1);
      }
#pragma unroll
      for (int j = 0; j < 4; ++j) { b4[j] = tr16(b8[j], b8[j + 4]); b4[j] += dpp<ROR8>(b4[j]); }
      if ((lane & 8) == 0) *LP(f32x4, L_RED + (tw * 128 + (lane >> 5) * 64 + 8 * (lane & 7) + 4 * (kg & 1)) * 4) = f32x4{b4[0], b4[1], b4[2], b4[3]};
      lds_arrive(L_QCNT, lane);
    }
    tag = (((u64)uni((unsigned)(gen >> 32)) << 32) | uni((unsigned)gen)) + 0x9E3779B97F4A7C15ull;
    if (tw == 0) {
      const u64 t48 = tag & ~0xFFFFull;
      if (qprod) {
        lds_wait<0>(L_QCNT, 7u);
#pragma unroll
        for (int b = 0; b < 2; ++b) {
          float qs = 0.f;
#pragma unroll
          for (int w = 0; w < 7; ++w) qs += *LP(float, L_RED + (w * 128 + b * 64 + lane) * 4);
          __hip_atomic_store((GAS u64*)(ws + W_QG) + b * HD + qc + lane, t48 | f2bf(qs), RLX_AGENT);
        }
      }
      lds_wait<2>(L_FULL, qprod ? 1 : QPOLL);
      GAS u64* qg = (GAS u64*)(ws + W_QG) + gi * DH + lane;
      u64 v[4];
#pragma unroll 1
      for (unsigned spins = 0; spins < SPIN_CAP; ++spins) {
        bool ok = true;
#pragma unroll
        for (int i = 0; i < 4; ++i) v[i] = __hip_atomic_load(qg + 64 * i, RLX_AGENT);
#pragma unroll
        for (int i = 0; i < 4; ++i) ok &= (v[i] & ~0xFFFFull) == t48;
        if (__all(ok)) break;
        __builtin_amdgcn_s_sleep(4);
      }
#pragma unroll
      for (int i = 0; i < 4; ++i) *LP(unsigned short, L_Q + (64 * i + lane) * 2) = (unsigned short)v[i];
      waitl();
      LV(unsigned, L_QRDY) = 1u;
    }
  }
  if (tw == 5) {
    lds_wait<2>(L_FULL, qprod ? 1 : 3);
    LV(unsigned, L_CORR) = LV(unsigned, L_E) - (qprod ? QLAG * 128 : 0);
  }
  u32x4 wf[2][8];
  if (roleB && tw < 4) {
    const __amdgpu_buffer_rsrc_t r = __builtin_amdgcn_make_buffer_rsrc((void*)wo, 0, HD * D * 2, 0x00020000);
    lds_wait<8>(L_FULL, TW);
    u32x4 raw[2][8];
#pragma unroll
    for (int s = 0; s < 2; ++s) ldtile(raw[s], r, (512 * ks + 128 * tw + 64 * s) * D + 64 * cs, tile_voff(lane));
#pragma unroll
    for (int s = 0; s < 2; ++s) {
      u32x4 p[8];
      pairrows(raw[s], p);
#pragma unroll
      for (int c = 0; c < 4; ++c)
#pragma unroll
        for (int jp = 0; jp < 4; ++jp) {
          wf[s][c][jp] = __builtin_amdgcn_update_dpp((int)p[c][jp], (int)p[4 + c][jp], ROR8, 0xf, 0xc, false);
          wf[s][4 + c][jp] = __builtin_amdgcn_update_dpp((int)p[4 + c][jp], (int)p[c][jp], ROR8, 0xf, 0x3, false);
        }
      pin8(wf[s]);
    }
  } else if (roleC) {
    const GAS char* gp = (const GAS char*)((n & 8) ? wu : wg) + ((64 * tw + 16 * kg) * FF + 64 * jc + 8 * (n & 7)) * 2;
    lds_wait<8>(L_FULL, tw == 5 ? 6 : TW);
    u32x4 raw[2][8];
#pragma unroll
    for (int j = 0; j < 16; ++j) raw[j >> 3][j & 7] = __builtin_nontemporal_load((const GAS u32x4*)(gp + j * (FF * 2)));
    __builtin_amdgcn_sched_barrier(0);
    pairrows(raw[0], wf[0]); pin8(wf[0]);
    pairrows(raw[1], wf[1]); pin8(wf[1]);
  }
  if (tw == 5) {
    const unsigned tag32 = (unsigned)(tag >> 32);
    const int adv = roleC ? ADVC : 0;
    unsigned A = uni(LV(unsigned, L_CORR)), prev = 0, e = 0;
    int Bd = 0, tk = 0, f = 0;
#pragma unroll 1
    for (int k2 = 0; k2 <= 2 * NCP; ++k2) {
      const int k = k2 >> 1, tc = CP0 + CPS * k;
      const int until = k == NCP ? NSTEP - 4 : (k2 & 1) ? tc + CPD + (gi & 3) : tc;
#pragma unroll 1
      while (f < until) {
        f = max((int)lds_wait<4>(L_FULL, f + 1), f + 1);
        unsigned tg = A + Bd * (f - tk) - adv * f;
        if (f >= 104) tg -= (int)(uni(LV(unsigned, L_E)) - tg) * 3 / 2;
        if (f >= 118) tg += 2 * (32768 / GAIN);
        LV(unsigned, L_CORR) = tg;
      }
      if (k == NCP) break;
      GAS u64* pp = (GAS u64*)(ws + W_PROG) + k * 32;
      if (!(k2 & 1)) {
        e = uni(LV(unsigned, L_E));
        if (lane == 0 && (g >> 5) == 7) __hip_atomic_store(pp + (g & 31), ((u64)tag32 << 32) | e, RLX_AGENT);
      } else {
        const u64 pv = __hip_atomic_load(pp + (lane & 31), RLX_AGENT);
        const bool ok = (unsigned)(pv >> 32) == tag32;
        const float sum = x16_sum(row16_sum(ok ? (float)(int)((unsigned)pv - e) : 0.f)), cnt = x16_sum(row16_sum(ok ? 1.f : 0.f));
        A = e + uni((unsigned)(int)(sum * __builtin_amdgcn_rcpf(fmaxf(cnt, 1.f))));
        Bd = k ? min(k == 1 ? 0 : BHI, (int)(A - prev) / CPS - BIAS) : 350;
        prev = A; tk = tc;
      }
    }
    LV(unsigned, L_TFIN) = A + (Bd + BIAS) * (NSTEP - tk) + NSTEP * TSCHED;
  }
  if (tw == 4) {
    lds_wait<8>(L_C2, 4u);
    const f32x4 lw = *LP(f32x4, L_LW);
    const float Mw = max4(*LP(f32x4, L_MW)), Lw = (lw[0] + lw[1]) + (lw[2] + lw[3]);
    const unsigned tlo = (unsigned)tag, thi = (unsigned)(tag >> 32);
    GAS u64* fp = (GAS u64*)(ws + W_FLAG);
    waitv<0>();
    if (lane < 2) __hip_atomic_store(fp + 2 * gl + lane, ((u64)(lane ? thi : tlo) << 32) | __float_as_uint(lane ? Lw : Mw), RLX_AGENT);
    lds_wait<16>(L_FULL, ML_STEP);
    const int nall = roleB || roleC ? 8 : 0;
    const unsigned want = (lane & 1) ? thi : tlo;
    u64 ml = 0;
#pragma unroll 1
    for (unsigned spins = 0; spins < SPIN_CAP; ++spins) {
      ml = __hip_atomic_load(fp + 16 * gi + n, RLX_AGENT);
      bool ok = (unsigned)(ml >> 32) == want;
#pragma unroll 1
      for (int i = 0; i < nall; i += 4) {
        u64 v[4];
#pragma unroll
        for (int j = 0; j < 4; ++j) v[j] = __hip_atomic_load(fp + 64 * (i + j) + lane, RLX_AGENT);
#pragma unroll
        for (int j = 0; j < 4; ++j) ok &= (unsigned)(v[j] >> 32) == want;
      }
      if (__all(ok)) break;
      __builtin_amdgcn_s_sleep(16);
    }
    const float mi = dpp<0xA0>(__uint_as_float((unsigned)ml)), li = dpp<0xF5>(__uint_as_float((unsigned)ml));
    float Mg = fmaxf(mi, dpp<XOR2>(mi)); Mg = fmaxf(Mg, dpp<HMIR>(Mg)); Mg = fmaxf(Mg, dpp<MIR>(Mg));
    float Lg = li * __builtin_amdgcn_exp2f(mi - Mg);
    Lg += dpp<XOR2>(Lg); Lg += dpp<HMIR>(Lg); Lg += dpp<MIR>(Lg);
    LV(float, L_SCALE) = __builtin_amdgcn_exp2f(Mw - Mg) / Lg;
    waitl();
    LV(unsigned, L_SCRDY) = 1u;
    if (g == 0 && lane == 0) __hip_atomic_store((GAS u64*)(ws + W_GEN), tag, RLX_AGENT);
  }

  if (!roleC) {
    if (!roleB || tw >= 4) return;
    unsigned off = W_RECA + (((kg & 1) * 16 + 2 * ks + (tw >> 1)) * 8 + (kg >> 1)) * RECA + 256 * (tw & 1) + 16 * n;
    unsigned ta = tw * 4352 + kg * 272 + 16 * n, tb = tw * 4352 + n * 272 + 16 * kg;
    unsigned wa = L_RED + ((tw * 2 + (lane >> 5)) * 64 + ccol(n) + 2 * (kg & 1)) * 4, ra = L_RED + (lane >> 5) * 1024 + (tw * 32 + (lane & 31)) * 4;
    int so = lane < 32 ? W_RECB + ((ks * 2 + (tw >> 1)) * D + 64 * cs + 32 * (tw & 1) + lane) * 4 : OOB;
    u32x4 v[4];
    v[3] = u32x4{0u, 0u, 0u, 0u};
    pin(v[3]); pin(off); pin(ta); pin(tb); pin(wa); pin(ra); pin(so);
    lds_wait<8>(L_SCRDY, 1u);
    ONE_PAGE(1280);
    lds_wait<8>(L_FULL, NSTEP - 3);
    lds_wait<1>(L_PUB, 4u);
    wait_expected(BX);
    gather4(v, off, ws, 2 * RECA);
#pragma unroll
    for (int i = 0; i < 4; ++i) *LP(u32x4, ta + i * 1088) = v[i];
    asm volatile("" ::: "memory");
#pragma unroll
    for (int i = 0; i < 4; ++i) v[i] = *LP(u32x4, tb + i * 64);
    __builtin_amdgcn_sched_barrier(0);
    f32x4 d[4];
#pragma unroll
    for (int c = 0; c < 4; ++c) d[c] = mfma(v[0], wf[0][c], f32x4{0.f, 0.f, 0.f, 0.f});
#pragma unroll
    for (int c = 0; c < 4; ++c) d[c] = mfma(v[1], wf[0][4 + c], d[c]);
    __builtin_amdgcn_sched_barrier(0);
#pragma unroll
    for (int s = 2; s < 4; ++s)
#pragma unroll
      for (int c = 0; c < 4; ++c) d[c] = mfma(v[s], wf[1][4 * (s & 1) + c], d[c]);
    __builtin_amdgcn_sched_barrier(0);
    float e[4];
#pragma unroll
    for (int c = 0; c < 4; ++c) e[c] = tr32(d[c][0] + d[c][2], d[c][1] + d[c][3]);
    *LP(f32x2, wa) = f32x2{tr16(e[0], e[2]), tr16(e[1], e[3])};
    bar();
    __builtin_amdgcn_raw_buffer_store_b32(canon(x32_sum(*LP(float, ra) + *LP(float, ra + 512))), rs, so, 0, AUX_SC1);
    return;
  }

  const int b = (n >> 2) & 1;
  const int col = 64 * tw + 16 * kg + 4 * (n & 3);
  const u32x2 xp = *(const GAS u32x2*)(x + b * D + col);
  f32x4 xf = {bflo(xp[0]), bfhi(xp[0]), bflo(xp[1]), bfhi(xp[1])};
  unsigned off = W_RECB + (((n & 8) + b) * D + col) * 4;
  unsigned a_h = n < 8 ? L_H + (b * D + col) * 2 : 32768 + tw * 512 + lane * 8;
  unsigned a_st = (lane & 0x1B) == 0 ? L_ST + (b * 8 + tw) * 8 + (lane >> 5) * 4 : 16384 + tw * 256 + lane * 4;
  unsigned a_red = n < 12 ? L_RED + tw * 1536 + (n >> 2) * 512 + (kg >> 1) * 256 + (kg & 1) * 16 + (n & 3) * 32 : tw * 2048 + lane * 16;
  unsigned a_rd = L_RED + tw * 512 + lane * 4, a_rc = L_RED + 1024 + lane * 4, a_sr = L_ST + (tw * 8 + (lane & 7)) * 8;
  unsigned a_g = L_G + (tw * 64 + 8 * (lane & 7) + (lane >> 3)) * 2, a_gr = L_G + (b * 64 + 32 * (n & 1) + 8 * kg) * 2;
  int so = lane < 32 ? W_RECC + ((jc * 2 + kg) * D + 64 * tw + ccol(n)) * 4 : OOB;
  unsigned msel = (lane & 8) ? ~0u : 0u;
  const int oc = 32 * jc + 4 * (lane & 7);
  unsigned offd = W_RECC + (lane >> 3) * (2 * D * 4) + oc * 4, a_hd = L_H + (oc + 2 * (lane >> 5)) * 2;
  int oo = (lane & 24) ? OOB : (oc + 2 * (lane >> 5)) * 2, vd = tile_voff(lane);
  constexpr unsigned ONE2 = 0x3F803F80u;
  u32x4 v[4], w[4], b0 = {ONE2, ONE2, ONE2, ONE2}, b1 = b0;
  v[3] = u32x4{0u, 0u, 0u, 0u}; w[3] = v[3];
  pin(v[3]); pin(w[3]); pin(b0); pin(b1); pin(xf); pin(off); pin(a_h); pin(a_st); pin(a_red); pin(a_rd); pin(a_rc); pin(a_sr);
  pin(a_g); pin(a_gr); pin(so); pin(msel); pin(offd); pin(a_hd); pin(oo); pin(vd);
  const __amdgpu_buffer_rsrc_t rd = __builtin_amdgcn_make_buffer_rsrc((void*)wd, 0, FF * D * 2, 0x00020000);
  const __amdgpu_buffer_rsrc_t ro = __builtin_amdgcn_make_buffer_rsrc((void*)out, 0, 2 * D * 2, 0x00020000);
  lds_wait<8>(L_SCRDY, 1u);
  ONE_PAGE(3840);
  lds_wait<8>(L_FULL, NSTEP - 3);
  lds_wait<1>(L_PUB, 4u);
  u32x4 fd[8];
  {
    u32x4 raw[8];
    ldtile(raw, rd, 64 * jc * D + 64 * tw, vd);
    pairrows(raw, fd); pin8(fd);
  }
  wait_expected(CX);
  gather4(v, off, ws, 2 * D * 4);
  {
    f32x4 a = sum4(v);
#pragma unroll
    for (int j = 0; j < 4; ++j) { float t = a[j] + dpp<ROR8>(a[j]); pin(t); a[j] = t; }
    unsigned a01 = pk2(a[0], a[1]), a23 = pk2(a[2], a[3]);
    asm volatile("" : "+v"(a01), "+v"(a23));
    const unsigned h0 = pk2(xf[0] + bflo(a01), xf[1] + bfhi(a01)), h1 = pk2(xf[2] + bflo(a23), xf[3] + bfhi(a23));
#define ACT(o, v, ctrl) o = (unsigned)__builtin_amdgcn_update_dpp((int)(o), (int)(v), ctrl, 0xf, 0x3, false)
    ACT(b0[0], h0, 0x00); ACT(b0[1], h1, 0x00); ACT(b0[2], h0, 0x55); ACT(b0[3], h1, 0x55);
    ACT(b1[0], h0, 0xAA); ACT(b1[1], h1, 0xAA); ACT(b1[2], h0, 0xFF); ACT(b1[3], h1, 0xFF);
    *LP(u32x2, a_h) = u32x2{h0, h1};
    f32x4 r[2];
#pragma unroll
    for (int c = 0; c < 8; ++c) {
      const f32x4 d = mfma(wf[1][c], b1, mfma(wf[0][c], b0, f32x4{0.f, 0.f, 0.f, 0.f}));
      if ((c & 3) == 0) r[c >> 2] = d;
      else {
        const bool take = __builtin_amdgcn_inverse_ballot_w64(0x1111111111111111ull << (c & 3));
#pragma unroll
        for (int j = 0; j < 4; ++j) r[c >> 2][j] = take ? d[j] : r[c >> 2][j];
      }
      if (c == 3) *LP(f32x4, a_red) = r[0];
    }
    float s1 = x16_sum(tr32(dot2(h1, ONE2, dot2(h0, ONE2, 0.f)), dot2(h1, h1, dot2(h0, h0, 0.f))));
    s1 += dpp<XOR1>(s1); s1 += dpp<XOR2>(s1);
    *LP(float, a_st) = s1;
    *LP(f32x4, a_red + 128) = r[1];
  }
  bar();
  if (tw < 2) {
    f32x2 st = *LP(f32x2, a_sr);
    f32x2 ps[7], pc[7];
#pragma unroll
    for (int i = 0; i < 7; ++i) {
      ps[i] = f32x2{*LP(float, a_rd + i * 1536), *LP(float, a_rd + i * 1536 + 256)};
      pc[i] = f32x2{*LP(float, a_rc + i * 1536), *LP(float, a_rc + i * 1536 + 256)};
    }
    __builtin_amdgcn_sched_barrier(0);
#pragma unroll
    for (int j = 0; j < 2; ++j) { st[j] += dpp<XOR1>(st[j]); st[j] += dpp<XOR2>(st[j]); st[j] += dpp<HMIR>(st[j]); }
    const float mean = st[0] * (1.f / D);
    const float mb = rbf(mean), rsd = ln_rstd(fmaxf(fmaf(-mean, mean, st[1] * (1.f / D)), 0.f));
    const f32x2 sm = ((ps[0] + ps[1]) + (ps[2] + ps[3])) + ((ps[4] + ps[5]) + ps[6]);
    const f32x2 cm = ((pc[0] + pc[1]) + (pc[2] + pc[3])) + ((pc[4] + pc[5]) + pc[6]);
    const unsigned gu = pk2((sm[0] - mb * cm[0]) * rsd, (sm[1] - mb * cm[1]) * rsd);
    const float xg = bflo(gu);
    constexpr float K1 = -2.f * 0.7978845608028654f * 1.4426950408889634f;
    const float ge = xg * __builtin_amdgcn_rcpf(1.f + __builtin_amdgcn_exp2f(xg * fmaf(xg * xg, K1 * 0.044715f, K1)));
    *LP(unsigned short, a_g) = (unsigned short)f2bf(rbf(ge) * bfhi(gu));
  }
  bar();
  {
    const u32x4 a0 = *LP(u32x4, a_gr);
    f32x4 d[8];
#pragma unroll
    for (int c = 0; c < 8; ++c) d[c] = mfma(a0, fd[c], f32x4{0.f, 0.f, 0.f, 0.f});
    __builtin_amdgcn_sched_barrier(0);
    u32x4 o;
#pragma unroll
    for (int c = 0; c < 4; ++c) {
      const unsigned keep = (__float_as_uint(d[c + 4][1]) & msel) | (__float_as_uint(d[c][0]) & ~msel);
      const unsigned send = (__float_as_uint(d[c][1]) & msel) | (__float_as_uint(d[c + 4][0]) & ~msel);
      o[c] = canon(__uint_as_float(keep) + dpp<ROR8>(__uint_as_float(send)));
    }
    __builtin_amdgcn_raw_buffer_store_b128(o, rs, so, 0, AUX_SC1);
  }
  if (tw == 6) {
    const unsigned hv = *LP(unsigned, a_hd);
    __builtin_amdgcn_s_sleep(DDELAY);
    gather4(w, offd, ws, 8 * (2 * D * 4), 0xFFFFFFFFull);
    const f32x4 t = sum4(w);
    float s0 = x16_sum(tr32(t[0], t[2])), s1 = x16_sum(tr32(t[1], t[3]));
    s0 += dpp<ROR8>(s0); s1 += dpp<ROR8>(s1);
    const unsigned dn = pk2(s0, s1);
    __builtin_amdgcn_raw_buffer_store_b32(pk2(bflo(hv) + bflo(dn), bfhi(hv) + bfhi(dn)), ro, oo, 0, 0);
  }
}

extern "C" void kernel_launch(void* const* d_in, const int* in_sizes, int n_in, void* d_out, int out_size, void* d_ws,
                              size_t ws_size, hipStream_t stream) {
  (void)hipFuncSetAttribute((const void*)fused_block, hipFuncAttributeMaxDynamicSharedMemorySize, LDS_BYTES);
  hipLaunchKernelGGL(fused_block, dim3(NWG), dim3(NTHR), LDS_BYTES, stream, (const bf16_t*)d_in[0], (const bf16_t*)d_in[1],
                     (const bf16_t*)d_in[2], (const bf16_t*)d_in[3], (const bf16_t*)d_in[4], (const bf16_t*)d_in[6],
                     (const bf16_t*)d_in[7], (const bf16_t*)d_in[8], (bf16_t*)d_out, (unsigned char*)d_ws);
}
```
